```python
import torch

def _softmax(x, axis=-1):
    return torch.softmax(x, dim=axis)
def _gelu(x):
    return torch.nn.functional.gelu(x, approximate="tanh")
def _silu(x):
    return torch.nn.functional.silu(x)
def _sigmoid(x):
    return torch.sigmoid(x)
def _layernorm(x, axis=-1, eps=1e-5):
    m = x.mean(dim=axis, keepdim=True)
    v = x.var(dim=axis, keepdim=True, unbiased=False)
    return (x - m) / torch.sqrt(v + eps)
def _scatter_add(tgt, idx, src, axis=0):
    return tgt.clone().index_add_(axis, idx, src)
def _segment_sum(src, seg, n):
    out = torch.zeros((n,) + src.shape[1:], dtype=src.dtype)
    return out.index_add_(0, seg, src)
def _pad(x, pw):
    flat = tuple(v for pair in reversed(pw) for v in pair)
    return torch.nn.functional.pad(x, flat)

kernel_name = 'gated_mix__softmax_full_mha__mixer_token_mlp__prompt'

def setup_inputs():

    _w = (lambda a: (lambda t: t.to(torch.bfloat16) if t.is_floating_point() else t)(torch.as_tensor(a)))
    def _A_setup():
        B, S, D = 1, 2048, 512
        return {
            "x": _w((lambda *s: torch.randn(*s, dtype=torch.bfloat16) * 0.05)(B, S, D)),
            "wqkv": _w((lambda *s: torch.randn(*s, dtype=torch.bfloat16) * 0.8)(D, 3 * D)),
            "wo": _w((lambda *s: torch.randn(*s, dtype=torch.bfloat16) * 0.05)(D, D)),
        }
    _A_in = _A_setup()

    def _B_setup():
        B, S, D, F = 1, 2048, 512, 1792
        return {
            "x": _w((lambda *s: torch.randn(*s, dtype=torch.bfloat16) * 0.05)(B, S, D)),
            "w1": _w((lambda *s: torch.randn(*s, dtype=torch.bfloat16) * 0.8)(S, F) * (S ** -0.5)),
            "b1": _w((lambda *s: torch.randn(*s, dtype=torch.bfloat16) * 12.8)(F) * 0.02),
            "w2": _w((lambda *s: torch.randn(*s, dtype=torch.bfloat16) * 0.05)(F, S) * (F ** -0.5)),
            "b2": _w((lambda *s: torch.randn(*s, dtype=torch.bfloat16) * 0.8)(S) * 0.02),
        }
    _B_in = _B_setup()

    return {
        "A_x": _A_in["x"],
        "A_wqkv": _A_in["wqkv"],
        "A_wo": _A_in["wo"],
        "B_x": _B_in["x"],
        "B_w1": _B_in["w1"],
        "B_b1": _B_in["b1"],
        "B_w2": _B_in["w2"],
        "B_b2": _B_in["b2"],
        "gate_w": _w((lambda *s: torch.randn(*s, dtype=torch.bfloat16) * 0.05)(512) * 16.0),
    }

def reference(A_x, A_wqkv, A_wo, B_x, B_w1, B_b1, B_w2, B_b2, gate_w):
    def _A_ref(x, wqkv, wo):
        B, S, D = x.shape
        H = 32; Dh = D // H
        qkv = (lambda x, *d: x.permute(*d))((x @ wqkv).reshape(B, S, 3, H, Dh), 2, 0, 3, 1, 4)
        q, k, v = qkv[0], qkv[1], qkv[2]
        scale = Dh ** -0.5
        scores = torch.einsum("bhqd,bhkd->bhqk", q, k) * scale
        p = _softmax(scores, axis=-1)
        o = torch.einsum("bhqk,bhkd->bhqd", p, v)
        return (lambda x, *d: x.permute(*d))(o, 0, 2, 1, 3).reshape(B, S, D) @ wo

    def _B_ref(x, w1, b1, w2, b2):
        B, S, D = x.shape
        h = _layernorm(x, axis=-1)
        u = torch.einsum("bsd,sf->bdf", h, w1) + b1
        u = _gelu(u)
        y = torch.einsum("bdf,fs->bds", u, w2) + b2
        return (lambda x, *d: x.permute(*d))(y, 0, 2, 1)

    x = A_x
    g = _sigmoid((x * gate_w).sum(-1, keepdims=True))
    a = _A_ref(x, A_wqkv, A_wo)
    b = _B_ref(x, B_w1, B_b1, B_w2, B_b2)
    return g * a + (1 - g) * b
```

```cpp
#include <hip/hip_runtime.h>
#include <hip/hip_bf16.h>
#include <stdint.h>

typedef unsigned short u16;
typedef unsigned long long u64;
typedef short bf16x8 __attribute__((ext_vector_type(8)));
typedef short s16x4 __attribute__((ext_vector_type(4)));
typedef float f32x16 __attribute__((ext_vector_type(16)));
typedef float f32x4 __attribute__((ext_vector_type(4)));
typedef float f32x2 __attribute__((ext_vector_type(2)));
typedef __bf16 bf2 __attribute__((ext_vector_type(2)));
typedef unsigned u32x4 __attribute__((ext_vector_type(4)));
typedef unsigned u32x2 __attribute__((ext_vector_type(2)));
typedef unsigned short u16x2 __attribute__((ext_vector_type(2)));
#define DI __device__ __forceinline__
#define LAS __attribute__((address_space(3)))
#define GAS __attribute__((address_space(1)))
#define MFMA(a, b, c) __builtin_amdgcn_mfma_f32_32x32x16_bf16((a), (b), (c), 0, 0, 0)

constexpr int CODE_LINES = 241;

constexpr int KVT = 2048, KVH = 64 * KVT;
constexpr unsigned WS_Q = 0, WS_KV = 2u << 20, WS_H = 7u << 20, WS_U = 9u << 20, WS_O = 11u << 20;
constexpr unsigned WS_KN = 13u << 20, WS_QN = WS_KN + 8192, WS_G = WS_QN + 8192;
constexpr unsigned WS_E = 14u << 20;
constexpr unsigned WS_F1 = WS_E + 4096, WS_F2 = WS_F1 + 48 * 64 * 8, WS_F3 = WS_F2 + 4 * 256 * 8, WS_F4 = WS_F3 + 8 * 28 * 4 * 8;
constexpr unsigned WS_END = WS_F4 + 32 * 32 * 8;
static_assert(WS_KV + 32 * KVH <= WS_H, "KV image");

constexpr int P1_SLOT = 28672, P1_NSLOT = 5;
constexpr int L_LN = P1_SLOT * P1_NSLOT;
constexpr int L_X = 0;
constexpr int G_DW = 4, G_RING = G_DW * 4096, L_RING = 57344;
constexpr int L_AR = 122880;
constexpr int L_CONST = L_AR + 32768, L_MISC = L_CONST + 1024, LDS_BYTES = L_MISC + 1024;
enum { M_TAG = 0, M_G = 16, C_FIRST = 64, C_R1B = C_FIRST, C_R1C, C_R2B, C_R2D, C_R3B, C_OA, C_OD, C_S0, C_S1, C_S2, C_S3, C_H1, C_AP, C_LND };
static_assert(L_AR % 8192 == 0 && L_LN + 9216 <= L_CONST && L_RING + 4 * G_RING <= L_AR && LDS_BYTES <= 163840, "LDS map");

#define ATTP_ASM \
  "s_mov_b32 %[m0s], m0\n\t" \
  "s_add_u32 m0, %[slot], 0\n\t" \
  "s_nop 0\n\t" \
  "global_load_lds_dwordx4 v232, s[40:41] sc1\n\t" \
  "global_load_lds_dwordx4 v232, s[40:41] offset:1024 sc1\n\t" \
  "s_add_u32 s40, s40, 2048\n\t" \
  "s_addc_u32 s41, s41, 0\n\t" \
  "s_add_u32 m0, %[slot], 2048\n\t" \
  "s_nop 0\n\t" \
  "global_load_lds_dwordx4 v232, s[40:41] sc1\n\t" \
  "global_load_lds_dwordx4 v232, s[40:41] offset:1024 sc1\n\t" \
  "s_add_u32 s40, s40, 2048\n\t" \
  "s_addc_u32 s41, s41, 0\n\t" \
  "s_add_u32 m0, %[slot], 6144\n\t" \
  "s_nop 0\n\t" \
  "global_load_lds_dwordx4 v232, s[40:41] sc1\n\t" \
  "global_load_lds_dwordx4 v232, s[40:41] offset:1024 sc1\n\t" \
  "s_add_u32 s40, s40, 2048\n\t" \
  "s_addc_u32 s41, s41, 0\n\t" \
  "s_add_u32 m0, %[slot], 4096\n\t" \
  "s_nop 0\n\t" \
  "global_load_lds_dwordx4 v232, s[40:41] sc1\n\t" \
  "global_load_lds_dwordx4 v232, s[40:41] offset:1024 sc1\n\t" \
  "s_add_u32 s40, s40, 2048\n\t" \
  "s_addc_u32 s41, s41, 0\n\t" \
  "s_mov_b32 m0, %[m0s]\n\t" \
  "v_mov_b32 v196, 0\n\t" \
  "v_mov_b32 v197, 0\n\t" \
  "v_mov_b32 v198, 0\n\t" \
  "v_mov_b32 v199, 0\n\t" \
  "s_nop 1\n\t" \
  "v_mfma_f32_32x32x16_bf16 v[0:15], v[196:199], v[196:199], 0\n\t" \
  "v_mfma_f32_32x32x16_bf16 v[16:31], v[196:199], v[196:199], 0\n\t" \
  "v_mfma_f32_32x32x16_bf16 v[32:47], v[196:199], v[196:199], 0\n\t" \
  "v_mfma_f32_32x32x16_bf16 v[48:63], v[196:199], v[196:199], 0\n\t" \
  "v_mfma_f32_32x32x16_bf16 v[64:79], v[196:199], v[196:199], 0\n\t" \
  "v_mfma_f32_32x32x16_bf16 v[80:95], v[196:199], v[196:199], 0\n\t" \
  "v_mfma_f32_32x32x16_bf16 v[96:111], v[196:199], v[196:199], 0\n\t" \
  "v_mfma_f32_32x32x16_bf16 v[112:127], v[196:199], v[196:199], 0\n\t" \
  "s_nop 7\n\t" \
  "s_nop 3\n\t" \
  ""
#define ATTP_OUTS "=&{v[0:15]}"(o[0]), "=&{v[16:31]}"(o[1]), "=&{v[32:47]}"(o[2]), "=&{v[48:63]}"(o[3]), "=&{v[64:79]}"(o[4]), "=&{v[80:95]}"(o[5]), "=&{v[96:111]}"(o[6]), "=&{v[112:127]}"(o[7]), "+{s[40:41]}"(kvp), [m0s] "=&s"(m0s)
#define ATTP_INS "{v232}"(dvo), [slot] "s"(slot)
#define ATTP_CLOB "v196", "v197", "v198", "v199", "scc", "memory"
#define ATT_ASM \
  "s_mov_b32 %[m0s], m0\n\t" \
  "s_waitcnt vmcnt(6)\n\t" \
  "ds_read_b128 v[200:203], v228\n\t" \
  "ds_read_b128 v[204:207], v229\n\t" \
  "v_mov_b32 v196, 0\n\t" \
  "v_mov_b32 v197, 0\n\t" \
  "v_mov_b32 v198, 0\n\t" \
  "v_mov_b32 v199, 0\n\t" \
  "v_mov_b32 v208, 0\n\t" \
  "v_mov_b32 v209, 0\n\t" \
  "v_mov_b32 v210, 0\n\t" \
  "v_mov_b32 v211, 0\n\t" \
  "s_mov_b32 %[cnt], 8\n\t" \
  "s_mov_b32 %[rem], 11\n\t" \
  "s_waitcnt lgkmcnt(0)\n\t" \
  "v_mfma_f32_32x32x16_bf16 v[160:175], v[200:203], v[128:131], v[212:227]\n\t" \
  "s_nop 7\n\t" \
  "s_nop 3\n\t" \
  "1:\n\t" \
  "v_exp_f32 v160, v160\n\t" \
  "v_exp_f32 v161, v161\n\t" \
  "v_mfma_f32_32x32x16_bf16 v[112:127], v[208:211], v[196:199], v[112:127]\n\t" \
  "ds_read_b128 v[208:211], v229 offset:512\n\t" \
  "v_exp_f32 v162, v162\n\t" \
  "v_cvt_pk_bf16_f32 v192, v160, v161\n\t" \
  "v_exp_f32 v163, v163\n\t" \
  "v_exp_f32 v164, v164\n\t" \
  "v_cvt_pk_bf16_f32 v193, v162, v163\n\t" \
  "v_exp_f32 v165, v165\n\t" \
  "v_exp_f32 v166, v166\n\t" \
  "v_mfma_f32_32x32x16_bf16 v[176:191], v[200:203], v[132:135], v[212:227]\n\t" \
  "s_waitcnt lgkmcnt(1)\n\t" \
  "v_cvt_pk_bf16_f32 v194, v164, v165\n\t" \
  "v_exp_f32 v167, v167\n\t" \
  "v_exp_f32 v168, v168\n\t" \
  "v_cvt_pk_bf16_f32 v195, v166, v167\n\t" \
  "v_exp_f32 v169, v169\n\t" \
  "v_exp_f32 v170, v170\n\t" \
  "v_mfma_f32_32x32x16_bf16 v[0:15], v[204:207], v[192:195], v[0:15]\n\t" \
  "s_waitcnt lgkmcnt(0)\n\t" \
  "v_cvt_pk_bf16_f32 v196, v168, v169\n\t" \
  "v_exp_f32 v171, v171\n\t" \
  "v_exp_f32 v172, v172\n\t" \
  "v_cvt_pk_bf16_f32 v197, v170, v171\n\t" \
  "v_exp_f32 v173, v173\n\t" \
  "v_exp_f32 v174, v174\n\t" \
  "v_exp_f32 v175, v175\n\t" \
  "v_cvt_pk_bf16_f32 v198, v172, v173\n\t" \
  "v_cvt_pk_bf16_f32 v199, v174, v175\n\t" \
  "v_exp_f32 v176, v176\n\t" \
  "v_exp_f32 v177, v177\n\t" \
  "v_mfma_f32_32x32x16_bf16 v[0:15], v[208:211], v[196:199], v[0:15]\n\t" \
  "s_mov_b32 m0, %[slot]\n\t" \
  "v_exp_f32 v178, v178\n\t" \
  "v_cvt_pk_bf16_f32 v192, v176, v177\n\t" \
  "v_exp_f32 v179, v179\n\t" \
  "v_exp_f32 v180, v180\n\t" \
  "v_cvt_pk_bf16_f32 v193, v178, v179\n\t" \
  "v_exp_f32 v181, v181\n\t" \
  "v_exp_f32 v182, v182\n\t" \
  "v_mfma_f32_32x32x16_bf16 v[160:175], v[200:203], v[136:139], v[212:227]\n\t" \
  "s_setprio 3\n\t" \
  "v_cvt_pk_bf16_f32 v194, v180, v181\n\t" \
  "v_exp_f32 v183, v183\n\t" \
  "v_exp_f32 v184, v184\n\t" \
  "v_cvt_pk_bf16_f32 v195, v182, v183\n\t" \
  "v_exp_f32 v185, v185\n\t" \
  "v_exp_f32 v186, v186\n\t" \
  "v_mfma_f32_32x32x16_bf16 v[16:31], v[204:207], v[192:195], v[16:31]\n\t" \
  "global_load_lds_dwordx4 v232, s[40:41] sc1\n\t" \
  "v_cvt_pk_bf16_f32 v196, v184, v185\n\t" \
  "v_exp_f32 v187, v187\n\t" \
  "v_exp_f32 v188, v188\n\t" \
  "v_cvt_pk_bf16_f32 v197, v186, v187\n\t" \
  "v_exp_f32 v189, v189\n\t" \
  "v_exp_f32 v190, v190\n\t" \
  "v_exp_f32 v191, v191\n\t" \
  "v_cvt_pk_bf16_f32 v198, v188, v189\n\t" \
  "v_cvt_pk_bf16_f32 v199, v190, v191\n\t" \
  "v_exp_f32 v160, v160\n\t" \
  "v_exp_f32 v161, v161\n\t" \
  "v_mfma_f32_32x32x16_bf16 v[16:31], v[208:211], v[196:199], v[16:31]\n\t" \
  "global_load_lds_dwordx4 v232, s[40:41] offset:1024 sc1\n\t" \
  "v_exp_f32 v162, v162\n\t" \
  "v_cvt_pk_bf16_f32 v192, v160, v161\n\t" \
  "v_exp_f32 v163, v163\n\t" \
  "v_exp_f32 v164, v164\n\t" \
  "v_cvt_pk_bf16_f32 v193, v162, v163\n\t" \
  "v_exp_f32 v165, v165\n\t" \
  "v_exp_f32 v166, v166\n\t" \
  "v_mfma_f32_32x32x16_bf16 v[176:191], v[200:203], v[140:143], v[212:227]\n\t" \
  "s_setprio 1\n\t" \
  "v_cvt_pk_bf16_f32 v194, v164, v165\n\t" \
  "v_exp_f32 v167, v167\n\t" \
  "v_exp_f32 v168, v168\n\t" \
  "v_cvt_pk_bf16_f32 v195, v166, v167\n\t" \
  "v_exp_f32 v169, v169\n\t" \
  "v_exp_f32 v170, v170\n\t" \
  "v_mfma_f32_32x32x16_bf16 v[32:47], v[204:207], v[192:195], v[32:47]\n\t" \
  "s_cmp_gt_i32 %[rem], 0\n\t" \
  "v_cvt_pk_bf16_f32 v196, v168, v169\n\t" \
  "v_exp_f32 v171, v171\n\t" \
  "v_exp_f32 v172, v172\n\t" \
  "v_cvt_pk_bf16_f32 v197, v170, v171\n\t" \
  "v_exp_f32 v173, v173\n\t" \
  "v_exp_f32 v174, v174\n\t" \
  "v_exp_f32 v175, v175\n\t" \
  "v_cvt_pk_bf16_f32 v198, v172, v173\n\t" \
  "v_cvt_pk_bf16_f32 v199, v174, v175\n\t" \
  "v_exp_f32 v176, v176\n\t" \
  "v_exp_f32 v177, v177\n\t" \
  "v_mfma_f32_32x32x16_bf16 v[32:47], v[208:211], v[196:199], v[32:47]\n\t" \
  "s_cselect_b32 %[inc], 2048, 0\n\t" \
  "v_exp_f32 v178, v178\n\t" \
  "v_cvt_pk_bf16_f32 v192, v176, v177\n\t" \
  "v_exp_f32 v179, v179\n\t" \
  "v_exp_f32 v180, v180\n\t" \
  "v_cvt_pk_bf16_f32 v193, v178, v179\n\t" \
  "v_exp_f32 v181, v181\n\t" \
  "v_exp_f32 v182, v182\n\t" \
  "v_mfma_f32_32x32x16_bf16 v[160:175], v[200:203], v[144:147], v[212:227]\n\t" \
  "s_sub_i32 %[rem], %[rem], 1\n\t" \
  "v_cvt_pk_bf16_f32 v194, v180, v181\n\t" \
  "v_exp_f32 v183, v183\n\t" \
  "v_exp_f32 v184, v184\n\t" \
  "v_cvt_pk_bf16_f32 v195, v182, v183\n\t" \
  "v_exp_f32 v185, v185\n\t" \
  "v_exp_f32 v186, v186\n\t" \
  "v_mfma_f32_32x32x16_bf16 v[48:63], v[204:207], v[192:195], v[48:63]\n\t" \
  "s_xor_b32 %[slot], 2048, %[slot]\n\t" \
  "v_cvt_pk_bf16_f32 v196, v184, v185\n\t" \
  "v_exp_f32 v187, v187\n\t" \
  "v_exp_f32 v188, v188\n\t" \
  "v_cvt_pk_bf16_f32 v197, v186, v187\n\t" \
  "v_exp_f32 v189, v189\n\t" \
  "v_exp_f32 v190, v190\n\t" \
  "v_exp_f32 v191, v191\n\t" \
  "v_cvt_pk_bf16_f32 v198, v188, v189\n\t" \
  "v_cvt_pk_bf16_f32 v199, v190, v191\n\t" \
  "v_exp_f32 v160, v160\n\t" \
  "v_exp_f32 v161, v161\n\t" \
  "v_mfma_f32_32x32x16_bf16 v[48:63], v[208:211], v[196:199], v[48:63]\n\t" \
  "s_add_u32 s40, s40, %[inc]\n\t" \
  "v_exp_f32 v162, v162\n\t" \
  "v_cvt_pk_bf16_f32 v192, v160, v161\n\t" \
  "v_exp_f32 v163, v163\n\t" \
  "v_exp_f32 v164, v164\n\t" \
  "v_cvt_pk_bf16_f32 v193, v162, v163\n\t" \
  "v_exp_f32 v165, v165\n\t" \
  "v_exp_f32 v166, v166\n\t" \
  "v_mfma_f32_32x32x16_bf16 v[176:191], v[200:203], v[148:151], v[212:227]\n\t" \
  "s_addc_u32 s41, s41, 0\n\t" \
  "v_cvt_pk_bf16_f32 v194, v164, v165\n\t" \
  "v_exp_f32 v167, v167\n\t" \
  "v_exp_f32 v168, v168\n\t" \
  "v_cvt_pk_bf16_f32 v195, v166, v167\n\t" \
  "v_exp_f32 v169, v169\n\t" \
  "v_exp_f32 v170, v170\n\t" \
  "v_mfma_f32_32x32x16_bf16 v[64:79], v[204:207], v[192:195], v[64:79]\n\t" \
  "v_cvt_pk_bf16_f32 v196, v168, v169\n\t" \
  "v_exp_f32 v171, v171\n\t" \
  "v_xor_b32 v228, 2048, v228\n\t" \
  "v_xor_b32 v229, v230, v229\n\t" \
  "v_exp_f32 v172, v172\n\t" \
  "v_cvt_pk_bf16_f32 v197, v170, v171\n\t" \
  "v_exp_f32 v173, v173\n\t" \
  "v_exp_f32 v174, v174\n\t" \
  "v_exp_f32 v175, v175\n\t" \
  "v_cvt_pk_bf16_f32 v198, v172, v173\n\t" \
  "v_cvt_pk_bf16_f32 v199, v174, v175\n\t" \
  "v_exp_f32 v176, v176\n\t" \
  "v_exp_f32 v177, v177\n\t" \
  "v_mfma_f32_32x32x16_bf16 v[64:79], v[208:211], v[196:199], v[64:79]\n\t" \
  "v_exp_f32 v178, v178\n\t" \
  "v_cvt_pk_bf16_f32 v192, v176, v177\n\t" \
  "v_exp_f32 v179, v179\n\t" \
  "v_exp_f32 v180, v180\n\t" \
  "v_cvt_pk_bf16_f32 v193, v178, v179\n\t" \
  "v_exp_f32 v181, v181\n\t" \
  "v_exp_f32 v182, v182\n\t" \
  "v_mfma_f32_32x32x16_bf16 v[160:175], v[200:203], v[152:155], v[212:227]\n\t" \
  "v_cvt_pk_bf16_f32 v194, v180, v181\n\t" \
  "v_exp_f32 v183, v183\n\t" \
  "v_exp_f32 v184, v184\n\t" \
  "v_cvt_pk_bf16_f32 v195, v182, v183\n\t" \
  "v_exp_f32 v185, v185\n\t" \
  "v_exp_f32 v186, v186\n\t" \
  "v_mfma_f32_32x32x16_bf16 v[80:95], v[204:207], v[192:195], v[80:95]\n\t" \
  "v_cvt_pk_bf16_f32 v196, v184, v185\n\t" \
  "v_exp_f32 v187, v187\n\t" \
  "v_exp_f32 v188, v188\n\t" \
  "v_cvt_pk_bf16_f32 v197, v186, v187\n\t" \
  "v_exp_f32 v189, v189\n\t" \
  "v_exp_f32 v190, v190\n\t" \
  "v_exp_f32 v191, v191\n\t" \
  "v_cvt_pk_bf16_f32 v198, v188, v189\n\t" \
  "v_cvt_pk_bf16_f32 v199, v190, v191\n\t" \
  "v_exp_f32 v160, v160\n\t" \
  "v_exp_f32 v161, v161\n\t" \
  "v_mfma_f32_32x32x16_bf16 v[80:95], v[208:211], v[196:199], v[80:95]\n\t" \
  "s_waitcnt vmcnt(6)\n\t" \
  "v_exp_f32 v162, v162\n\t" \
  "v_cvt_pk_bf16_f32 v192, v160, v161\n\t" \
  "v_exp_f32 v163, v163\n\t" \
  "v_exp_f32 v164, v164\n\t" \
  "v_cvt_pk_bf16_f32 v193, v162, v163\n\t" \
  "v_exp_f32 v165, v165\n\t" \
  "v_exp_f32 v166, v166\n\t" \
  "v_mfma_f32_32x32x16_bf16 v[176:191], v[200:203], v[156:159], v[212:227]\n\t" \
  "ds_read_b128 v[200:203], v228\n\t" \
  "v_cvt_pk_bf16_f32 v194, v164, v165\n\t" \
  "v_exp_f32 v167, v167\n\t" \
  "v_exp_f32 v168, v168\n\t" \
  "v_cvt_pk_bf16_f32 v195, v166, v167\n\t" \
  "v_exp_f32 v169, v169\n\t" \
  "v_exp_f32 v170, v170\n\t" \
  "v_mfma_f32_32x32x16_bf16 v[96:111], v[204:207], v[192:195], v[96:111]\n\t" \
  "v_cvt_pk_bf16_f32 v196, v168, v169\n\t" \
  "v_exp_f32 v171, v171\n\t" \
  "v_exp_f32 v172, v172\n\t" \
  "v_cvt_pk_bf16_f32 v197, v170, v171\n\t" \
  "v_exp_f32 v173, v173\n\t" \
  "v_exp_f32 v174, v174\n\t" \
  "v_exp_f32 v175, v175\n\t" \
  "v_cvt_pk_bf16_f32 v198, v172, v173\n\t" \
  "v_cvt_pk_bf16_f32 v199, v174, v175\n\t" \
  "v_exp_f32 v176, v176\n\t" \
  "v_exp_f32 v177, v177\n\t" \
  "v_mfma_f32_32x32x16_bf16 v[96:111], v[208:211], v[196:199], v[96:111]\n\t" \
  "s_waitcnt lgkmcnt(0)\n\t" \
  "v_exp_f32 v178, v178\n\t" \
  "v_cvt_pk_bf16_f32 v192, v176, v177\n\t" \
  "v_exp_f32 v179, v179\n\t" \
  "v_exp_f32 v180, v180\n\t" \
  "v_cvt_pk_bf16_f32 v193, v178, v179\n\t" \
  "v_exp_f32 v181, v181\n\t" \
  "v_exp_f32 v182, v182\n\t" \
  "v_mfma_f32_32x32x16_bf16 v[160:175], v[200:203], v[128:131], v[212:227]\n\t" \
  "v_cvt_pk_bf16_f32 v194, v180, v181\n\t" \
  "v_exp_f32 v183, v183\n\t" \
  "v_exp_f32 v184, v184\n\t" \
  "v_cvt_pk_bf16_f32 v195, v182, v183\n\t" \
  "v_exp_f32 v185, v185\n\t" \
  "v_exp_f32 v186, v186\n\t" \
  "v_mfma_f32_32x32x16_bf16 v[112:127], v[204:207], v[192:195], v[112:127]\n\t" \
  "ds_read_b128 v[204:207], v229\n\t" \
  "v_cvt_pk_bf16_f32 v196, v184, v185\n\t" \
  "v_exp_f32 v187, v187\n\t" \
  "v_exp_f32 v188, v188\n\t" \
  "v_cvt_pk_bf16_f32 v197, v186, v187\n\t" \
  "v_exp_f32 v189, v189\n\t" \
  "v_exp_f32 v190, v190\n\t" \
  "v_exp_f32 v191, v191\n\t" \
  "v_cvt_pk_bf16_f32 v198, v188, v189\n\t" \
  "v_cvt_pk_bf16_f32 v199, v190, v191\n\t" \
  "v_exp_f32 v160, v160\n\t" \
  "v_exp_f32 v161, v161\n\t" \
  "v_mfma_f32_32x32x16_bf16 v[112:127], v[208:211], v[196:199], v[112:127]\n\t" \
  "ds_read_b128 v[208:211], v229 offset:512\n\t" \
  "v_exp_f32 v162, v162\n\t" \
  "v_cvt_pk_bf16_f32 v192, v160, v161\n\t" \
  "v_exp_f32 v163, v163\n\t" \
  "v_exp_f32 v164, v164\n\t" \
  "v_cvt_pk_bf16_f32 v193, v162, v163\n\t" \
  "v_exp_f32 v165, v165\n\t" \
  "v_exp_f32 v166, v166\n\t" \
  "v_mfma_f32_32x32x16_bf16 v[176:191], v[200:203], v[132:135], v[212:227]\n\t" \
  "s_waitcnt lgkmcnt(1)\n\t" \
  "v_cvt_pk_bf16_f32 v194, v164, v165\n\t" \
  "v_exp_f32 v167, v167\n\t" \
  "v_exp_f32 v168, v168\n\t" \
  "v_cvt_pk_bf16_f32 v195, v166, v167\n\t" \
  "v_exp_f32 v169, v169\n\t" \
  "v_exp_f32 v170, v170\n\t" \
  "v_mfma_f32_32x32x16_bf16 v[0:15], v[204:207], v[192:195], v[0:15]\n\t" \
  "s_waitcnt lgkmcnt(0)\n\t" \
  "v_cvt_pk_bf16_f32 v196, v168, v169\n\t" \
  "v_exp_f32 v171, v171\n\t" \
  "v_exp_f32 v172, v172\n\t" \
  "v_cvt_pk_bf16_f32 v197, v170, v171\n\t" \
  "v_exp_f32 v173, v173\n\t" \
  "v_exp_f32 v174, v174\n\t" \
  "v_exp_f32 v175, v175\n\t" \
  "v_cvt_pk_bf16_f32 v198, v172, v173\n\t" \
  "v_cvt_pk_bf16_f32 v199, v174, v175\n\t" \
  "v_exp_f32 v176, v176\n\t" \
  "v_exp_f32 v177, v177\n\t" \
  "v_mfma_f32_32x32x16_bf16 v[0:15], v[208:211], v[196:199], v[0:15]\n\t" \
  "s_mov_b32 m0, %[slot]\n\t" \
  "v_exp_f32 v178, v178\n\t" \
  "v_cvt_pk_bf16_f32 v192, v176, v177\n\t" \
  "v_exp_f32 v179, v179\n\t" \
  "v_exp_f32 v180, v180\n\t" \
  "v_cvt_pk_bf16_f32 v193, v178, v179\n\t" \
  "v_exp_f32 v181, v181\n\t" \
  "v_exp_f32 v182, v182\n\t" \
  "v_mfma_f32_32x32x16_bf16 v[160:175], v[200:203], v[136:139], v[212:227]\n\t" \
  "s_setprio 3\n\t" \
  "v_cvt_pk_bf16_f32 v194, v180, v181\n\t" \
  "v_exp_f32 v183, v183\n\t" \
  "v_exp_f32 v184, v184\n\t" \
  "v_cvt_pk_bf16_f32 v195, v182, v183\n\t" \
  "v_exp_f32 v185, v185\n\t" \
  "v_exp_f32 v186, v186\n\t" \
  "v_mfma_f32_32x32x16_bf16 v[16:31], v[204:207], v[192:195], v[16:31]\n\t" \
  "global_load_lds_dwordx4 v232, s[40:41] sc1\n\t" \
  "v_cvt_pk_bf16_f32 v196, v184, v185\n\t" \
  "v_exp_f32 v187, v187\n\t" \
  "v_exp_f32 v188, v188\n\t" \
  "v_cvt_pk_bf16_f32 v197, v186, v187\n\t" \
  "v_exp_f32 v189, v189\n\t" \
  "v_exp_f32 v190, v190\n\t" \
  "v_exp_f32 v191, v191\n\t" \
  "v_cvt_pk_bf16_f32 v198, v188, v189\n\t" \
  "v_cvt_pk_bf16_f32 v199, v190, v191\n\t" \
  "v_exp_f32 v160, v160\n\t" \
  "v_exp_f32 v161, v161\n\t" \
  "v_mfma_f32_32x32x16_bf16 v[16:31], v[208:211], v[196:199], v[16:31]\n\t" \
  "global_load_lds_dwordx4 v232, s[40:41] offset:1024 sc1\n\t" \
  "v_exp_f32 v162, v162\n\t" \
  "v_cvt_pk_bf16_f32 v192, v160, v161\n\t" \
  "v_exp_f32 v163, v163\n\t" \
  "v_exp_f32 v164, v164\n\t" \
  "v_cvt_pk_bf16_f32 v193, v162, v163\n\t" \
  "v_exp_f32 v165, v165\n\t" \
  "v_exp_f32 v166, v166\n\t" \
  "v_mfma_f32_32x32x16_bf16 v[176:191], v[200:203], v[140:143], v[212:227]\n\t" \
  "s_setprio 1\n\t" \
  "v_cvt_pk_bf16_f32 v194, v164, v165\n\t" \
  "v_exp_f32 v167, v167\n\t" \
  "v_exp_f32 v168, v168\n\t" \
  "v_cvt_pk_bf16_f32 v195, v166, v167\n\t" \
  "v_exp_f32 v169, v169\n\t" \
  "v_exp_f32 v170, v170\n\t" \
  "v_mfma_f32_32x32x16_bf16 v[32:47], v[204:207], v[192:195], v[32:47]\n\t" \
  "s_cmp_gt_i32 %[rem], 0\n\t" \
  "v_cvt_pk_bf16_f32 v196, v168, v169\n\t" \
  "v_exp_f32 v171, v171\n\t" \
  "v_exp_f32 v172, v172\n\t" \
  "v_cvt_pk_bf16_f32 v197, v170, v171\n\t" \
  "v_exp_f32 v173, v173\n\t" \
  "v_exp_f32 v174, v174\n\t" \
  "v_exp_f32 v175, v175\n\t" \
  "v_cvt_pk_bf16_f32 v198, v172, v173\n\t" \
  "v_cvt_pk_bf16_f32 v199, v174, v175\n\t" \
  "v_exp_f32 v176, v176\n\t" \
  "v_exp_f32 v177, v177\n\t" \
  "v_mfma_f32_32x32x16_bf16 v[32:47], v[208:211], v[196:199], v[32:47]\n\t" \
  "s_cselect_b32 %[inc], 2048, 0\n\t" \
  "v_exp_f32 v178, v178\n\t" \
  "v_cvt_pk_bf16_f32 v192, v176, v177\n\t" \
  "v_exp_f32 v179, v179\n\t" \
  "v_exp_f32 v180, v180\n\t" \
  "v_cvt_pk_bf16_f32 v193, v178, v179\n\t" \
  "v_exp_f32 v181, v181\n\t" \
  "v_exp_f32 v182, v182\n\t" \
  "v_mfma_f32_32x32x16_bf16 v[160:175], v[200:203], v[144:147], v[212:227]\n\t" \
  "s_sub_i32 %[rem], %[rem], 1\n\t" \
  "v_cvt_pk_bf16_f32 v194, v180, v181\n\t" \
  "v_exp_f32 v183, v183\n\t" \
  "v_exp_f32 v184, v184\n\t" \
  "v_cvt_pk_bf16_f32 v195, v182, v183\n\t" \
  "v_exp_f32 v185, v185\n\t" \
  "v_exp_f32 v186, v186\n\t" \
  "v_mfma_f32_32x32x16_bf16 v[48:63], v[204:207], v[192:195], v[48:63]\n\t" \
  "s_xor_b32 %[slot], 4096, %[slot]\n\t" \
  "v_cvt_pk_bf16_f32 v196, v184, v185\n\t" \
  "v_exp_f32 v187, v187\n\t" \
  "v_exp_f32 v188, v188\n\t" \
  "v_cvt_pk_bf16_f32 v197, v186, v187\n\t" \
  "v_exp_f32 v189, v189\n\t" \
  "v_exp_f32 v190, v190\n\t" \
  "v_exp_f32 v191, v191\n\t" \
  "v_cvt_pk_bf16_f32 v198, v188, v189\n\t" \
  "v_cvt_pk_bf16_f32 v199, v190, v191\n\t" \
  "v_exp_f32 v160, v160\n\t" \
  "v_exp_f32 v161, v161\n\t" \
  "v_mfma_f32_32x32x16_bf16 v[48:63], v[208:211], v[196:199], v[48:63]\n\t" \
  "s_add_u32 s40, s40, %[inc]\n\t" \
  "v_exp_f32 v162, v162\n\t" \
  "v_cvt_pk_bf16_f32 v192, v160, v161\n\t" \
  "v_exp_f32 v163, v163\n\t" \
  "v_exp_f32 v164, v164\n\t" \
  "v_cvt_pk_bf16_f32 v193, v162, v163\n\t" \
  "v_exp_f32 v165, v165\n\t" \
  "v_exp_f32 v166, v166\n\t" \
  "v_mfma_f32_32x32x16_bf16 v[176:191], v[200:203], v[148:151], v[212:227]\n\t" \
  "s_addc_u32 s41, s41, 0\n\t" \
  "v_cvt_pk_bf16_f32 v194, v164, v165\n\t" \
  "v_exp_f32 v167, v167\n\t" \
  "v_exp_f32 v168, v168\n\t" \
  "v_cvt_pk_bf16_f32 v195, v166, v167\n\t" \
  "v_exp_f32 v169, v169\n\t" \
  "v_exp_f32 v170, v170\n\t" \
  "v_mfma_f32_32x32x16_bf16 v[64:79], v[204:207], v[192:195], v[64:79]\n\t" \
  "v_cvt_pk_bf16_f32 v196, v168, v169\n\t" \
  "v_exp_f32 v171, v171\n\t" \
  "v_xor_b32 v228, 4096, v228\n\t" \
  "v_xor_b32 v229, v231, v229\n\t" \
  "v_exp_f32 v172, v172\n\t" \
  "v_cvt_pk_bf16_f32 v197, v170, v171\n\t" \
  "v_exp_f32 v173, v173\n\t" \
  "v_exp_f32 v174, v174\n\t" \
  "v_exp_f32 v175, v175\n\t" \
  "v_cvt_pk_bf16_f32 v198, v172, v173\n\t" \
  "v_cvt_pk_bf16_f32 v199, v174, v175\n\t" \
  "v_exp_f32 v176, v176\n\t" \
  "v_exp_f32 v177, v177\n\t" \
  "v_mfma_f32_32x32x16_bf16 v[64:79], v[208:211], v[196:199], v[64:79]\n\t" \
  "s_sub_u32 %[cnt], %[cnt], 1\n\t" \
  "v_exp_f32 v178, v178\n\t" \
  "v_cvt_pk_bf16_f32 v192, v176, v177\n\t" \
  "v_exp_f32 v179, v179\n\t" \
  "v_exp_f32 v180, v180\n\t" \
  "v_cvt_pk_bf16_f32 v193, v178, v179\n\t" \
  "v_exp_f32 v181, v181\n\t" \
  "v_exp_f32 v182, v182\n\t" \
  "v_mfma_f32_32x32x16_bf16 v[160:175], v[200:203], v[152:155], v[212:227]\n\t" \
  "v_cvt_pk_bf16_f32 v194, v180, v181\n\t" \
  "v_exp_f32 v183, v183\n\t" \
  "v_exp_f32 v184, v184\n\t" \
  "v_cvt_pk_bf16_f32 v195, v182, v183\n\t" \
  "v_exp_f32 v185, v185\n\t" \
  "v_exp_f32 v186, v186\n\t" \
  "v_mfma_f32_32x32x16_bf16 v[80:95], v[204:207], v[192:195], v[80:95]\n\t" \
  "v_cvt_pk_bf16_f32 v196, v184, v185\n\t" \
  "v_exp_f32 v187, v187\n\t" \
  "v_exp_f32 v188, v188\n\t" \
  "v_cvt_pk_bf16_f32 v197, v186, v187\n\t" \
  "v_exp_f32 v189, v189\n\t" \
  "v_exp_f32 v190, v190\n\t" \
  "v_exp_f32 v191, v191\n\t" \
  "v_cvt_pk_bf16_f32 v198, v188, v189\n\t" \
  "v_cvt_pk_bf16_f32 v199, v190, v191\n\t" \
  "v_exp_f32 v160, v160\n\t" \
  "v_exp_f32 v161, v161\n\t" \
  "v_mfma_f32_32x32x16_bf16 v[80:95], v[208:211], v[196:199], v[80:95]\n\t" \
  "s_waitcnt vmcnt(6)\n\t" \
  "v_exp_f32 v162, v162\n\t" \
  "v_cvt_pk_bf16_f32 v192, v160, v161\n\t" \
  "v_exp_f32 v163, v163\n\t" \
  "v_exp_f32 v164, v164\n\t" \
  "v_cvt_pk_bf16_f32 v193, v162, v163\n\t" \
  "v_exp_f32 v165, v165\n\t" \
  "v_exp_f32 v166, v166\n\t" \
  "v_mfma_f32_32x32x16_bf16 v[176:191], v[200:203], v[156:159], v[212:227]\n\t" \
  "ds_read_b128 v[200:203], v228\n\t" \
  "v_cvt_pk_bf16_f32 v194, v164, v165\n\t" \
  "v_exp_f32 v167, v167\n\t" \
  "v_exp_f32 v168, v168\n\t" \
  "v_cvt_pk_bf16_f32 v195, v166, v167\n\t" \
  "v_exp_f32 v169, v169\n\t" \
  "v_exp_f32 v170, v170\n\t" \
  "v_mfma_f32_32x32x16_bf16 v[96:111], v[204:207], v[192:195], v[96:111]\n\t" \
  "v_cvt_pk_bf16_f32 v196, v168, v169\n\t" \
  "v_exp_f32 v171, v171\n\t" \
  "v_exp_f32 v172, v172\n\t" \
  "v_cvt_pk_bf16_f32 v197, v170, v171\n\t" \
  "v_exp_f32 v173, v173\n\t" \
  "v_exp_f32 v174, v174\n\t" \
  "v_exp_f32 v175, v175\n\t" \
  "v_cvt_pk_bf16_f32 v198, v172, v173\n\t" \
  "v_cvt_pk_bf16_f32 v199, v174, v175\n\t" \
  "v_exp_f32 v176, v176\n\t" \
  "v_exp_f32 v177, v177\n\t" \
  "v_mfma_f32_32x32x16_bf16 v[96:111], v[208:211], v[196:199], v[96:111]\n\t" \
  "s_waitcnt lgkmcnt(0)\n\t" \
  "v_exp_f32 v178, v178\n\t" \
  "v_cvt_pk_bf16_f32 v192, v176, v177\n\t" \
  "v_exp_f32 v179, v179\n\t" \
  "v_exp_f32 v180, v180\n\t" \
  "v_cvt_pk_bf16_f32 v193, v178, v179\n\t" \
  "v_exp_f32 v181, v181\n\t" \
  "v_exp_f32 v182, v182\n\t" \
  "v_mfma_f32_32x32x16_bf16 v[160:175], v[200:203], v[128:131], v[212:227]\n\t" \
  "s_cmp_lg_u32 %[cnt], 0\n\t" \
  "v_cvt_pk_bf16_f32 v194, v180, v181\n\t" \
  "v_exp_f32 v183, v183\n\t" \
  "v_exp_f32 v184, v184\n\t" \
  "v_cvt_pk_bf16_f32 v195, v182, v183\n\t" \
  "v_exp_f32 v185, v185\n\t" \
  "v_exp_f32 v186, v186\n\t" \
  "v_mfma_f32_32x32x16_bf16 v[112:127], v[204:207], v[192:195], v[112:127]\n\t" \
  "ds_read_b128 v[204:207], v229\n\t" \
  "v_cvt_pk_bf16_f32 v196, v184, v185\n\t" \
  "v_exp_f32 v187, v187\n\t" \
  "v_exp_f32 v188, v188\n\t" \
  "v_cvt_pk_bf16_f32 v197, v186, v187\n\t" \
  "v_exp_f32 v189, v189\n\t" \
  "v_exp_f32 v190, v190\n\t" \
  "v_exp_f32 v191, v191\n\t" \
  "v_cvt_pk_bf16_f32 v198, v188, v189\n\t" \
  "v_cvt_pk_bf16_f32 v199, v190, v191\n\t" \
  "s_cbranch_scc1 1b\n\t" \
  "s_nop 1\n\t" \
  "v_mfma_f32_32x32x16_bf16 v[112:127], v[208:211], v[196:199], v[112:127]\n\t" \
  "s_waitcnt vmcnt(0) lgkmcnt(0)\n\t" \
  "s_mov_b32 m0, %[m0s]\n\t" \
  "s_nop 7\n\t" \
  "s_nop 3\n\t" \
  ""
#define ATT_OUTS "+{v[0:15]}"(o[0]), "+{v[16:31]}"(o[1]), "+{v[32:47]}"(o[2]), "+{v[48:63]}"(o[3]), "+{v[64:79]}"(o[4]), "+{v[80:95]}"(o[5]), "+{v[96:111]}"(o[6]), "+{v[112:127]}"(o[7]), "+{s[40:41]}"(kvp), "+{v228}"(ka), "+{v229}"(va), [slot] "+s"(slot), [cnt] "=&s"(cnt), [inc] "=&s"(inc), [rem] "=&s"(rem), [m0s] "=&s"(m0s)
#define ATT_INS "{v[212:227]}"(negm), "{v[128:131]}"(q[0]), "{v[132:135]}"(q[1]), "{v[136:139]}"(q[2]), "{v[140:143]}"(q[3]), "{v[144:147]}"(q[4]), "{v[148:151]}"(q[5]), "{v[152:155]}"(q[6]), "{v[156:159]}"(q[7]), "{v230}"(xm1), "{v231}"(xm2), "{v232}"(dvo)
#define ATT_CLOB "v160", "v161", "v162", "v163", "v164", "v165", "v166", "v167", "v168", "v169", "v170", "v171", "v172", "v173", "v174", "v175", "v176", "v177", "v178", "v179", "v180", "v181", "v182", "v183", "v184", "v185", "v186", "v187", "v188", "v189", "v190", "v191", "v200", "v201", "v202", "v203", "v204", "v205", "v206", "v207", "v208", "v209", "v210", "v211", "v192", "v193", "v194", "v195", "v196", "v197", "v198", "v199", "scc", "memory"
#define G1P_ASM \
  "s_mov_b32 %[m0s], m0\n\t" \
  "s_add_u32 m0, %[ldsb], 0\n\t" \
  "s_nop 0\n\t" \
  "global_load_lds_dwordx4 %[wo0], s[40:41] offset:0\n\t" \
  "global_load_lds_dwordx4 %[wo1], s[40:41] offset:1024\n\t" \
  "global_load_lds_dwordx4 %[wo2], s[40:41] offset:2048\n\t" \
  "global_load_lds_dwordx4 %[wo3], s[40:41] offset:3072\n\t" \
  "s_add_u32 s40, s40, 114688\n\t" \
  "s_addc_u32 s41, s41, 0\n\t" \
  "s_add_u32 m0, %[ldsb], 4096\n\t" \
  "s_nop 0\n\t" \
  "global_load_lds_dwordx4 %[wo0], s[40:41] offset:0\n\t" \
  "global_load_lds_dwordx4 %[wo1], s[40:41] offset:1024\n\t" \
  "global_load_lds_dwordx4 %[wo2], s[40:41] offset:2048\n\t" \
  "global_load_lds_dwordx4 %[wo3], s[40:41] offset:3072\n\t" \
  "s_add_u32 s40, s40, 114688\n\t" \
  "s_addc_u32 s41, s41, 0\n\t" \
  "s_add_u32 m0, %[ldsb], 8192\n\t" \
  "s_nop 0\n\t" \
  "global_load_lds_dwordx4 %[wo0], s[40:41] offset:0\n\t" \
  "global_load_lds_dwordx4 %[wo1], s[40:41] offset:1024\n\t" \
  "global_load_lds_dwordx4 %[wo2], s[40:41] offset:2048\n\t" \
  "global_load_lds_dwordx4 %[wo3], s[40:41] offset:3072\n\t" \
  "s_add_u32 s40, s40, 114688\n\t" \
  "s_addc_u32 s41, s41, 0\n\t" \
  "s_add_u32 m0, %[ldsb], 12288\n\t" \
  "s_nop 0\n\t" \
  "global_load_lds_dwordx4 %[wo0], s[40:41] offset:0\n\t" \
  "global_load_lds_dwordx4 %[wo1], s[40:41] offset:1024\n\t" \
  "global_load_lds_dwordx4 %[wo2], s[40:41] offset:2048\n\t" \
  "global_load_lds_dwordx4 %[wo3], s[40:41] offset:3072\n\t" \
  "s_add_u32 s40, s40, 114688\n\t" \
  "s_addc_u32 s41, s41, 0\n\t" \
  "s_mov_b32 m0, %[m0s]\n\t" \
  ""
#define G1_ASM \
  "s_mov_b32 %[m0s], m0\n\t" \
  "global_load_dwordx4 v[96:99], %[xo0], s[42:43] offset:0 sc1\n\t" \
  "global_load_dwordx4 v[100:103], %[xo1], s[42:43] offset:0 sc1\n\t" \
  "global_load_dwordx4 v[104:107], %[xo0], s[42:43] offset:1024 sc1\n\t" \
  "global_load_dwordx4 v[108:111], %[xo1], s[42:43] offset:1024 sc1\n\t" \
  "s_add_u32 s42, s42, 2048\n\t" \
  "s_addc_u32 s43, s43, 0\n\t" \
  "global_load_dwordx4 v[112:115], %[xo0], s[42:43] offset:0 sc1\n\t" \
  "global_load_dwordx4 v[116:119], %[xo1], s[42:43] offset:0 sc1\n\t" \
  "global_load_dwordx4 v[120:123], %[xo0], s[42:43] offset:1024 sc1\n\t" \
  "global_load_dwordx4 v[124:127], %[xo1], s[42:43] offset:1024 sc1\n\t" \
  "s_add_u32 s42, s42, 2048\n\t" \
  "s_addc_u32 s43, s43, 0\n\t" \
  "global_load_dwordx4 v[128:131], %[xo0], s[42:43] offset:0 sc1\n\t" \
  "global_load_dwordx4 v[132:135], %[xo1], s[42:43] offset:0 sc1\n\t" \
  "global_load_dwordx4 v[136:139], %[xo0], s[42:43] offset:1024 sc1\n\t" \
  "global_load_dwordx4 v[140:143], %[xo1], s[42:43] offset:1024 sc1\n\t" \
  "s_add_u32 s42, s42, 2048\n\t" \
  "s_addc_u32 s43, s43, 0\n\t" \
  "global_load_dwordx4 v[144:147], %[xo0], s[42:43] offset:0 sc1\n\t" \
  "global_load_dwordx4 v[148:151], %[xo1], s[42:43] offset:0 sc1\n\t" \
  "global_load_dwordx4 v[152:155], %[xo0], s[42:43] offset:1024 sc1\n\t" \
  "global_load_dwordx4 v[156:159], %[xo1], s[42:43] offset:1024 sc1\n\t" \
  "s_add_u32 s42, s42, 2048\n\t" \
  "s_addc_u32 s43, s43, 0\n\t" \
  "v_mov_b32 v80, 0\n\t" \
  "v_mov_b32 v81, 0\n\t" \
  "v_mov_b32 v82, 0\n\t" \
  "v_mov_b32 v83, 0\n\t" \
  "s_nop 1\n\t" \
  "v_mfma_f32_32x32x16_bf16 v[0:15], v[80:83], v[80:83], 0\n\t" \
  "v_mfma_f32_32x32x16_bf16 v[16:31], v[80:83], v[80:83], 0\n\t" \
  "v_mfma_f32_32x32x16_bf16 v[32:47], v[80:83], v[80:83], 0\n\t" \
  "v_mfma_f32_32x32x16_bf16 v[48:63], v[80:83], v[80:83], 0\n\t" \
  "s_waitcnt vmcnt(8)\n\t" \
  "ds_read_b64_tr_b16 v[64:65], %[lr0] offset:0\n\t" \
  "ds_read_b64_tr_b16 v[66:67], %[lr0] offset:512\n\t" \
  "ds_read_b64_tr_b16 v[68:69], %[lr1] offset:0\n\t" \
  "ds_read_b64_tr_b16 v[70:71], %[lr1] offset:512\n\t" \
  "ds_read_b64_tr_b16 v[72:73], %[lr0] offset:2048\n\t" \
  "ds_read_b64_tr_b16 v[74:75], %[lr0] offset:2560\n\t" \
  "ds_read_b64_tr_b16 v[76:77], %[lr1] offset:2048\n\t" \
  "ds_read_b64_tr_b16 v[78:79], %[lr1] offset:2560\n\t" \
  "2:\n\t" \
  "s_waitcnt lgkmcnt(0)\n\t" \
  "s_add_u32 m0, %[ldsb], 0\n\t" \
  "s_nop 0\n\t" \
  "global_load_lds_dwordx4 %[wo0], s[40:41] offset:0\n\t" \
  "global_load_lds_dwordx4 %[wo1], s[40:41] offset:1024\n\t" \
  "global_load_lds_dwordx4 %[wo2], s[40:41] offset:2048\n\t" \
  "global_load_lds_dwordx4 %[wo3], s[40:41] offset:3072\n\t" \
  "s_add_u32 s40, s40, 114688\n\t" \
  "s_addc_u32 s41, s41, 0\n\t" \
  "s_waitcnt vmcnt(24)\n\t" \
  "ds_read_b64_tr_b16 v[80:81], %[lr0] offset:4096\n\t" \
  "ds_read_b64_tr_b16 v[82:83], %[lr0] offset:4608\n\t" \
  "ds_read_b64_tr_b16 v[84:85], %[lr1] offset:4096\n\t" \
  "ds_read_b64_tr_b16 v[86:87], %[lr1] offset:4608\n\t" \
  "ds_read_b64_tr_b16 v[88:89], %[lr0] offset:6144\n\t" \
  "ds_read_b64_tr_b16 v[90:91], %[lr0] offset:6656\n\t" \
  "ds_read_b64_tr_b16 v[92:93], %[lr1] offset:6144\n\t" \
  "ds_read_b64_tr_b16 v[94:95], %[lr1] offset:6656\n\t" \
  "s_setprio 0\n\t" \
  "v_mfma_f32_32x32x16_bf16 v[0:15], v[64:67], v[96:99], v[0:15]\n\t" \
  "v_mfma_f32_32x32x16_bf16 v[16:31], v[64:67], v[100:103], v[16:31]\n\t" \
  "v_mfma_f32_32x32x16_bf16 v[32:47], v[68:71], v[96:99], v[32:47]\n\t" \
  "v_mfma_f32_32x32x16_bf16 v[48:63], v[68:71], v[100:103], v[48:63]\n\t" \
  "v_mfma_f32_32x32x16_bf16 v[0:15], v[72:75], v[104:107], v[0:15]\n\t" \
  "v_mfma_f32_32x32x16_bf16 v[16:31], v[72:75], v[108:111], v[16:31]\n\t" \
  "v_mfma_f32_32x32x16_bf16 v[32:47], v[76:79], v[104:107], v[32:47]\n\t" \
  "v_mfma_f32_32x32x16_bf16 v[48:63], v[76:79], v[108:111], v[48:63]\n\t" \
  "s_setprio 2\n\t" \
  "global_load_dwordx4 v[96:99], %[xo0], s[42:43] offset:0 sc1\n\t" \
  "global_load_dwordx4 v[100:103], %[xo1], s[42:43] offset:0 sc1\n\t" \
  "global_load_dwordx4 v[104:107], %[xo0], s[42:43] offset:1024 sc1\n\t" \
  "global_load_dwordx4 v[108:111], %[xo1], s[42:43] offset:1024 sc1\n\t" \
  "s_add_u32 s42, s42, 2048\n\t" \
  "s_addc_u32 s43, s43, 0\n\t" \
  "s_waitcnt lgkmcnt(0)\n\t" \
  "s_add_u32 m0, %[ldsb], 4096\n\t" \
  "s_nop 0\n\t" \
  "global_load_lds_dwordx4 %[wo0], s[40:41] offset:0\n\t" \
  "global_load_lds_dwordx4 %[wo1], s[40:41] offset:1024\n\t" \
  "global_load_lds_dwordx4 %[wo2], s[40:41] offset:2048\n\t" \
  "global_load_lds_dwordx4 %[wo3], s[40:41] offset:3072\n\t" \
  "s_add_u32 s40, s40, 114688\n\t" \
  "s_addc_u32 s41, s41, 0\n\t" \
  "s_waitcnt vmcnt(24)\n\t" \
  "ds_read_b64_tr_b16 v[64:65], %[lr0] offset:8192\n\t" \
  "ds_read_b64_tr_b16 v[66:67], %[lr0] offset:8704\n\t" \
  "ds_read_b64_tr_b16 v[68:69], %[lr1] offset:8192\n\t" \
  "ds_read_b64_tr_b16 v[70:71], %[lr1] offset:8704\n\t" \
  "ds_read_b64_tr_b16 v[72:73], %[lr0] offset:10240\n\t" \
  "ds_read_b64_tr_b16 v[74:75], %[lr0] offset:10752\n\t" \
  "ds_read_b64_tr_b16 v[76:77], %[lr1] offset:10240\n\t" \
  "ds_read_b64_tr_b16 v[78:79], %[lr1] offset:10752\n\t" \
  "s_setprio 0\n\t" \
  "v_mfma_f32_32x32x16_bf16 v[0:15], v[80:83], v[112:115], v[0:15]\n\t" \
  "v_mfma_f32_32x32x16_bf16 v[16:31], v[80:83], v[116:119], v[16:31]\n\t" \
  "v_mfma_f32_32x32x16_bf16 v[32:47], v[84:87], v[112:115], v[32:47]\n\t" \
  "v_mfma_f32_32x32x16_bf16 v[48:63], v[84:87], v[116:119], v[48:63]\n\t" \
  "v_mfma_f32_32x32x16_bf16 v[0:15], v[88:91], v[120:123], v[0:15]\n\t" \
  "v_mfma_f32_32x32x16_bf16 v[16:31], v[88:91], v[124:127], v[16:31]\n\t" \
  "v_mfma_f32_32x32x16_bf16 v[32:47], v[92:95], v[120:123], v[32:47]\n\t" \
  "v_mfma_f32_32x32x16_bf16 v[48:63], v[92:95], v[124:127], v[48:63]\n\t" \
  "s_setprio 2\n\t" \
  "global_load_dwordx4 v[112:115], %[xo0], s[42:43] offset:0 sc1\n\t" \
  "global_load_dwordx4 v[116:119], %[xo1], s[42:43] offset:0 sc1\n\t" \
  "global_load_dwordx4 v[120:123], %[xo0], s[42:43] offset:1024 sc1\n\t" \
  "global_load_dwordx4 v[124:127], %[xo1], s[42:43] offset:1024 sc1\n\t" \
  "s_add_u32 s42, s42, 2048\n\t" \
  "s_addc_u32 s43, s43, 0\n\t" \
  "s_waitcnt lgkmcnt(0)\n\t" \
  "s_add_u32 m0, %[ldsb], 8192\n\t" \
  "s_nop 0\n\t" \
  "global_load_lds_dwordx4 %[wo0], s[40:41] offset:0\n\t" \
  "global_load_lds_dwordx4 %[wo1], s[40:41] offset:1024\n\t" \
  "global_load_lds_dwordx4 %[wo2], s[40:41] offset:2048\n\t" \
  "global_load_lds_dwordx4 %[wo3], s[40:41] offset:3072\n\t" \
  "s_add_u32 s40, s40, 114688\n\t" \
  "s_addc_u32 s41, s41, 0\n\t" \
  "s_waitcnt vmcnt(24)\n\t" \
  "ds_read_b64_tr_b16 v[80:81], %[lr0] offset:12288\n\t" \
  "ds_read_b64_tr_b16 v[82:83], %[lr0] offset:12800\n\t" \
  "ds_read_b64_tr_b16 v[84:85], %[lr1] offset:12288\n\t" \
  "ds_read_b64_tr_b16 v[86:87], %[lr1] offset:12800\n\t" \
  "ds_read_b64_tr_b16 v[88:89], %[lr0] offset:14336\n\t" \
  "ds_read_b64_tr_b16 v[90:91], %[lr0] offset:14848\n\t" \
  "ds_read_b64_tr_b16 v[92:93], %[lr1] offset:14336\n\t" \
  "ds_read_b64_tr_b16 v[94:95], %[lr1] offset:14848\n\t" \
  "s_setprio 0\n\t" \
  "v_mfma_f32_32x32x16_bf16 v[0:15], v[64:67], v[128:131], v[0:15]\n\t" \
  "v_mfma_f32_32x32x16_bf16 v[16:31], v[64:67], v[132:135], v[16:31]\n\t" \
  "v_mfma_f32_32x32x16_bf16 v[32:47], v[68:71], v[128:131], v[32:47]\n\t" \
  "v_mfma_f32_32x32x16_bf16 v[48:63], v[68:71], v[132:135], v[48:63]\n\t" \
  "v_mfma_f32_32x32x16_bf16 v[0:15], v[72:75], v[136:139], v[0:15]\n\t" \
  "v_mfma_f32_32x32x16_bf16 v[16:31], v[72:75], v[140:143], v[16:31]\n\t" \
  "v_mfma_f32_32x32x16_bf16 v[32:47], v[76:79], v[136:139], v[32:47]\n\t" \
  "v_mfma_f32_32x32x16_bf16 v[48:63], v[76:79], v[140:143], v[48:63]\n\t" \
  "s_setprio 2\n\t" \
  "global_load_dwordx4 v[128:131], %[xo0], s[42:43] offset:0 sc1\n\t" \
  "global_load_dwordx4 v[132:135], %[xo1], s[42:43] offset:0 sc1\n\t" \
  "global_load_dwordx4 v[136:139], %[xo0], s[42:43] offset:1024 sc1\n\t" \
  "global_load_dwordx4 v[140:143], %[xo1], s[42:43] offset:1024 sc1\n\t" \
  "s_add_u32 s42, s42, 2048\n\t" \
  "s_addc_u32 s43, s43, 0\n\t" \
  "s_waitcnt lgkmcnt(0)\n\t" \
  "s_add_u32 m0, %[ldsb], 12288\n\t" \
  "s_nop 0\n\t" \
  "global_load_lds_dwordx4 %[wo0], s[40:41] offset:0\n\t" \
  "global_load_lds_dwordx4 %[wo1], s[40:41] offset:1024\n\t" \
  "global_load_lds_dwordx4 %[wo2], s[40:41] offset:2048\n\t" \
  "global_load_lds_dwordx4 %[wo3], s[40:41] offset:3072\n\t" \
  "s_add_u32 s40, s40, 114688\n\t" \
  "s_addc_u32 s41, s41, 0\n\t" \
  "s_waitcnt vmcnt(24)\n\t" \
  "ds_read_b64_tr_b16 v[64:65], %[lr0] offset:0\n\t" \
  "ds_read_b64_tr_b16 v[66:67], %[lr0] offset:512\n\t" \
  "ds_read_b64_tr_b16 v[68:69], %[lr1] offset:0\n\t" \
  "ds_read_b64_tr_b16 v[70:71], %[lr1] offset:512\n\t" \
  "ds_read_b64_tr_b16 v[72:73], %[lr0] offset:2048\n\t" \
  "ds_read_b64_tr_b16 v[74:75], %[lr0] offset:2560\n\t" \
  "ds_read_b64_tr_b16 v[76:77], %[lr1] offset:2048\n\t" \
  "ds_read_b64_tr_b16 v[78:79], %[lr1] offset:2560\n\t" \
  "s_setprio 0\n\t" \
  "v_mfma_f32_32x32x16_bf16 v[0:15], v[80:83], v[144:147], v[0:15]\n\t" \
  "v_mfma_f32_32x32x16_bf16 v[16:31], v[80:83], v[148:151], v[16:31]\n\t" \
  "v_mfma_f32_32x32x16_bf16 v[32:47], v[84:87], v[144:147], v[32:47]\n\t" \
  "v_mfma_f32_32x32x16_bf16 v[48:63], v[84:87], v[148:151], v[48:63]\n\t" \
  "v_mfma_f32_32x32x16_bf16 v[0:15], v[88:91], v[152:155], v[0:15]\n\t" \
  "v_mfma_f32_32x32x16_bf16 v[16:31], v[88:91], v[156:159], v[16:31]\n\t" \
  "v_mfma_f32_32x32x16_bf16 v[32:47], v[92:95], v[152:155], v[32:47]\n\t" \
  "v_mfma_f32_32x32x16_bf16 v[48:63], v[92:95], v[156:159], v[48:63]\n\t" \
  "s_setprio 2\n\t" \
  "global_load_dwordx4 v[144:147], %[xo0], s[42:43] offset:0 sc1\n\t" \
  "global_load_dwordx4 v[148:151], %[xo1], s[42:43] offset:0 sc1\n\t" \
  "global_load_dwordx4 v[152:155], %[xo0], s[42:43] offset:1024 sc1\n\t" \
  "global_load_dwordx4 v[156:159], %[xo1], s[42:43] offset:1024 sc1\n\t" \
  "s_add_u32 s42, s42, 2048\n\t" \
  "s_addc_u32 s43, s43, 0\n\t" \
  "s_sub_u32 %[cnt], %[cnt], 1\n\t" \
  "s_cmp_lg_u32 %[cnt], 0\n\t" \
  "s_cbranch_scc1 2b\n\t" \
  "s_waitcnt lgkmcnt(0)\n\t" \
  "s_waitcnt vmcnt(20)\n\t" \
  "ds_read_b64_tr_b16 v[80:81], %[lr0] offset:4096\n\t" \
  "ds_read_b64_tr_b16 v[82:83], %[lr0] offset:4608\n\t" \
  "ds_read_b64_tr_b16 v[84:85], %[lr1] offset:4096\n\t" \
  "ds_read_b64_tr_b16 v[86:87], %[lr1] offset:4608\n\t" \
  "ds_read_b64_tr_b16 v[88:89], %[lr0] offset:6144\n\t" \
  "ds_read_b64_tr_b16 v[90:91], %[lr0] offset:6656\n\t" \
  "ds_read_b64_tr_b16 v[92:93], %[lr1] offset:6144\n\t" \
  "ds_read_b64_tr_b16 v[94:95], %[lr1] offset:6656\n\t" \
  "s_setprio 0\n\t" \
  "v_mfma_f32_32x32x16_bf16 v[0:15], v[64:67], v[96:99], v[0:15]\n\t" \
  "v_mfma_f32_32x32x16_bf16 v[16:31], v[64:67], v[100:103], v[16:31]\n\t" \
  "v_mfma_f32_32x32x16_bf16 v[32:47], v[68:71], v[96:99], v[32:47]\n\t" \
  "v_mfma_f32_32x32x16_bf16 v[48:63], v[68:71], v[100:103], v[48:63]\n\t" \
  "v_mfma_f32_32x32x16_bf16 v[0:15], v[72:75], v[104:107], v[0:15]\n\t" \
  "v_mfma_f32_32x32x16_bf16 v[16:31], v[72:75], v[108:111], v[16:31]\n\t" \
  "v_mfma_f32_32x32x16_bf16 v[32:47], v[76:79], v[104:107], v[32:47]\n\t" \
  "v_mfma_f32_32x32x16_bf16 v[48:63], v[76:79], v[108:111], v[48:63]\n\t" \
  "s_setprio 2\n\t" \
  "s_waitcnt lgkmcnt(0)\n\t" \
  "s_waitcnt vmcnt(12)\n\t" \
  "ds_read_b64_tr_b16 v[64:65], %[lr0] offset:8192\n\t" \
  "ds_read_b64_tr_b16 v[66:67], %[lr0] offset:8704\n\t" \
  "ds_read_b64_tr_b16 v[68:69], %[lr1] offset:8192\n\t" \
  "ds_read_b64_tr_b16 v[70:71], %[lr1] offset:8704\n\t" \
  "ds_read_b64_tr_b16 v[72:73], %[lr0] offset:10240\n\t" \
  "ds_read_b64_tr_b16 v[74:75], %[lr0] offset:10752\n\t" \
  "ds_read_b64_tr_b16 v[76:77], %[lr1] offset:10240\n\t" \
  "ds_read_b64_tr_b16 v[78:79], %[lr1] offset:10752\n\t" \
  "s_setprio 0\n\t" \
  "v_mfma_f32_32x32x16_bf16 v[0:15], v[80:83], v[112:115], v[0:15]\n\t" \
  "v_mfma_f32_32x32x16_bf16 v[16:31], v[80:83], v[116:119], v[16:31]\n\t" \
  "v_mfma_f32_32x32x16_bf16 v[32:47], v[84:87], v[112:115], v[32:47]\n\t" \
  "v_mfma_f32_32x32x16_bf16 v[48:63], v[84:87], v[116:119], v[48:63]\n\t" \
  "v_mfma_f32_32x32x16_bf16 v[0:15], v[88:91], v[120:123], v[0:15]\n\t" \
  "v_mfma_f32_32x32x16_bf16 v[16:31], v[88:91], v[124:127], v[16:31]\n\t" \
  "v_mfma_f32_32x32x16_bf16 v[32:47], v[92:95], v[120:123], v[32:47]\n\t" \
  "v_mfma_f32_32x32x16_bf16 v[48:63], v[92:95], v[124:127], v[48:63]\n\t" \
  "s_setprio 2\n\t" \
  "s_waitcnt lgkmcnt(0)\n\t" \
  "s_waitcnt vmcnt(4)\n\t" \
  "ds_read_b64_tr_b16 v[80:81], %[lr0] offset:12288\n\t" \
  "ds_read_b64_tr_b16 v[82:83], %[lr0] offset:12800\n\t" \
  "ds_read_b64_tr_b16 v[84:85], %[lr1] offset:12288\n\t" \
  "ds_read_b64_tr_b16 v[86:87], %[lr1] offset:12800\n\t" \
  "ds_read_b64_tr_b16 v[88:89], %[lr0] offset:14336\n\t" \
  "ds_read_b64_tr_b16 v[90:91], %[lr0] offset:14848\n\t" \
  "ds_read_b64_tr_b16 v[92:93], %[lr1] offset:14336\n\t" \
  "ds_read_b64_tr_b16 v[94:95], %[lr1] offset:14848\n\t" \
  "s_setprio 0\n\t" \
  "v_mfma_f32_32x32x16_bf16 v[0:15], v[64:67], v[128:131], v[0:15]\n\t" \
  "v_mfma_f32_32x32x16_bf16 v[16:31], v[64:67], v[132:135], v[16:31]\n\t" \
  "v_mfma_f32_32x32x16_bf16 v[32:47], v[68:71], v[128:131], v[32:47]\n\t" \
  "v_mfma_f32_32x32x16_bf16 v[48:63], v[68:71], v[132:135], v[48:63]\n\t" \
  "v_mfma_f32_32x32x16_bf16 v[0:15], v[72:75], v[136:139], v[0:15]\n\t" \
  "v_mfma_f32_32x32x16_bf16 v[16:31], v[72:75], v[140:143], v[16:31]\n\t" \
  "v_mfma_f32_32x32x16_bf16 v[32:47], v[76:79], v[136:139], v[32:47]\n\t" \
  "v_mfma_f32_32x32x16_bf16 v[48:63], v[76:79], v[140:143], v[48:63]\n\t" \
  "s_setprio 2\n\t" \
  "s_waitcnt lgkmcnt(0)\n\t" \
  "s_waitcnt vmcnt(0)\n\t" \
  "s_setprio 0\n\t" \
  "v_mfma_f32_32x32x16_bf16 v[0:15], v[80:83], v[144:147], v[0:15]\n\t" \
  "v_mfma_f32_32x32x16_bf16 v[16:31], v[80:83], v[148:151], v[16:31]\n\t" \
  "v_mfma_f32_32x32x16_bf16 v[32:47], v[84:87], v[144:147], v[32:47]\n\t" \
  "v_mfma_f32_32x32x16_bf16 v[48:63], v[84:87], v[148:151], v[48:63]\n\t" \
  "v_mfma_f32_32x32x16_bf16 v[0:15], v[88:91], v[152:155], v[0:15]\n\t" \
  "v_mfma_f32_32x32x16_bf16 v[16:31], v[88:91], v[156:159], v[16:31]\n\t" \
  "v_mfma_f32_32x32x16_bf16 v[32:47], v[92:95], v[152:155], v[32:47]\n\t" \
  "v_mfma_f32_32x32x16_bf16 v[48:63], v[92:95], v[156:159], v[48:63]\n\t" \
  "s_setprio 2\n\t" \
  "s_mov_b32 m0, %[m0s]\n\t" \
  "s_nop 7\n\t" \
  "s_nop 3\n\t" \
  ""
#define G1_CLOB "v64", "v65", "v66", "v67", "v68", "v69", "v70", "v71", "v72", "v73", "v74", "v75", "v76", "v77", "v78", "v79", "v80", "v81", "v82", "v83", "v84", "v85", "v86", "v87", "v88", "v89", "v90", "v91", "v92", "v93", "v94", "v95", "v96", "v97", "v98", "v99", "v100", "v101", "v102", "v103", "v104", "v105", "v106", "v107", "v108", "v109", "v110", "v111", "v112", "v113", "v114", "v115", "v116", "v117", "v118", "v119", "v120", "v121", "v122", "v123", "v124", "v125", "v126", "v127", "v128", "v129", "v130", "v131", "v132", "v133", "v134", "v135", "v136", "v137", "v138", "v139", "v140", "v141", "v142", "v143", "v144", "v145", "v146", "v147", "v148", "v149", "v150", "v151", "v152", "v153", "v154", "v155", "v156", "v157", "v158", "v159"
#define G2P_ASM \
  "s_mov_b32 %[m0s], m0\n\t" \
  "s_add_u32 m0, %[ldsb], 0\n\t" \
  "s_nop 0\n\t" \
  "global_load_lds_dwordx4 %[wo0], s[40:41] offset:0\n\t" \
  "global_load_lds_dwordx4 %[wo1], s[40:41] offset:1024\n\t" \
  "global_load_lds_dwordx4 %[wo2], s[40:41] offset:2048\n\t" \
  "global_load_lds_dwordx4 %[wo3], s[40:41] offset:3072\n\t" \
  "s_add_u32 s40, s40, 131072\n\t" \
  "s_addc_u32 s41, s41, 0\n\t" \
  "s_add_u32 m0, %[ldsb], 4096\n\t" \
  "s_nop 0\n\t" \
  "global_load_lds_dwordx4 %[wo0], s[40:41] offset:0\n\t" \
  "global_load_lds_dwordx4 %[wo1], s[40:41] offset:1024\n\t" \
  "global_load_lds_dwordx4 %[wo2], s[40:41] offset:2048\n\t" \
  "global_load_lds_dwordx4 %[wo3], s[40:41] offset:3072\n\t" \
  "s_add_u32 s40, s40, 131072\n\t" \
  "s_addc_u32 s41, s41, 0\n\t" \
  "s_add_u32 m0, %[ldsb], 8192\n\t" \
  "s_nop 0\n\t" \
  "global_load_lds_dwordx4 %[wo0], s[40:41] offset:0\n\t" \
  "global_load_lds_dwordx4 %[wo1], s[40:41] offset:1024\n\t" \
  "global_load_lds_dwordx4 %[wo2], s[40:41] offset:2048\n\t" \
  "global_load_lds_dwordx4 %[wo3], s[40:41] offset:3072\n\t" \
  "s_add_u32 s40, s40, 131072\n\t" \
  "s_addc_u32 s41, s41, 0\n\t" \
  "s_add_u32 m0, %[ldsb], 12288\n\t" \
  "s_nop 0\n\t" \
  "global_load_lds_dwordx4 %[wo0], s[40:41] offset:0\n\t" \
  "global_load_lds_dwordx4 %[wo1], s[40:41] offset:1024\n\t" \
  "global_load_lds_dwordx4 %[wo2], s[40:41] offset:2048\n\t" \
  "global_load_lds_dwordx4 %[wo3], s[40:41] offset:3072\n\t" \
  "s_add_u32 s40, s40, 131072\n\t" \
  "s_addc_u32 s41, s41, 0\n\t" \
  "s_mov_b32 m0, %[m0s]\n\t" \
  ""
#define G2_ASM \
  "s_mov_b32 %[m0s], m0\n\t" \
  "global_load_dwordx4 v[96:99], %[xo0], s[42:43] offset:0 sc1\n\t" \
  "global_load_dwordx4 v[100:103], %[xo1], s[42:43] offset:0 sc1\n\t" \
  "global_load_dwordx4 v[104:107], %[xo0], s[42:43] offset:1024 sc1\n\t" \
  "global_load_dwordx4 v[108:111], %[xo1], s[42:43] offset:1024 sc1\n\t" \
  "s_add_u32 s42, s42, 2048\n\t" \
  "s_addc_u32 s43, s43, 0\n\t" \
  "global_load_dwordx4 v[112:115], %[xo0], s[42:43] offset:0 sc1\n\t" \
  "global_load_dwordx4 v[116:119], %[xo1], s[42:43] offset:0 sc1\n\t" \
  "global_load_dwordx4 v[120:123], %[xo0], s[42:43] offset:1024 sc1\n\t" \
  "global_load_dwordx4 v[124:127], %[xo1], s[42:43] offset:1024 sc1\n\t" \
  "s_add_u32 s42, s42, 2048\n\t" \
  "s_addc_u32 s43, s43, 0\n\t" \
  "global_load_dwordx4 v[128:131], %[xo0], s[42:43] offset:0 sc1\n\t" \
  "global_load_dwordx4 v[132:135], %[xo1], s[42:43] offset:0 sc1\n\t" \
  "global_load_dwordx4 v[136:139], %[xo0], s[42:43] offset:1024 sc1\n\t" \
  "global_load_dwordx4 v[140:143], %[xo1], s[42:43] offset:1024 sc1\n\t" \
  "s_add_u32 s42, s42, 2048\n\t" \
  "s_addc_u32 s43, s43, 0\n\t" \
  "global_load_dwordx4 v[144:147], %[xo0], s[42:43] offset:0 sc1\n\t" \
  "global_load_dwordx4 v[148:151], %[xo1], s[42:43] offset:0 sc1\n\t" \
  "global_load_dwordx4 v[152:155], %[xo0], s[42:43] offset:1024 sc1\n\t" \
  "global_load_dwordx4 v[156:159], %[xo1], s[42:43] offset:1024 sc1\n\t" \
  "s_add_u32 s42, s42, 2048\n\t" \
  "s_addc_u32 s43, s43, 0\n\t" \
  "v_mov_b32 v80, 0\n\t" \
  "v_mov_b32 v81, 0\n\t" \
  "v_mov_b32 v82, 0\n\t" \
  "v_mov_b32 v83, 0\n\t" \
  "s_nop 1\n\t" \
  "v_mfma_f32_32x32x16_bf16 v[0:15], v[80:83], v[80:83], 0\n\t" \
  "v_mfma_f32_32x32x16_bf16 v[16:31], v[80:83], v[80:83], 0\n\t" \
  "v_mfma_f32_32x32x16_bf16 v[32:47], v[80:83], v[80:83], 0\n\t" \
  "v_mfma_f32_32x32x16_bf16 v[48:63], v[80:83], v[80:83], 0\n\t" \
  "s_waitcnt vmcnt(8)\n\t" \
  "ds_read_b64_tr_b16 v[64:65], %[lr0] offset:0\n\t" \
  "ds_read_b64_tr_b16 v[66:67], %[lr0] offset:1024\n\t" \
  "ds_read_b64_tr_b16 v[68:69], %[lr1] offset:0\n\t" \
  "ds_read_b64_tr_b16 v[70:71], %[lr1] offset:1024\n\t" \
  "ds_read_b64_tr_b16 v[72:73], %[lr0] offset:2048\n\t" \
  "ds_read_b64_tr_b16 v[74:75], %[lr0] offset:3072\n\t" \
  "ds_read_b64_tr_b16 v[76:77], %[lr1] offset:2048\n\t" \
  "ds_read_b64_tr_b16 v[78:79], %[lr1] offset:3072\n\t" \
  "2:\n\t" \
  "s_waitcnt lgkmcnt(0)\n\t" \
  "s_add_u32 m0, %[ldsb], 0\n\t" \
  "s_nop 0\n\t" \
  "global_load_lds_dwordx4 %[wo0], s[40:41] offset:0\n\t" \
  "global_load_lds_dwordx4 %[wo1], s[40:41] offset:1024\n\t" \
  "global_load_lds_dwordx4 %[wo2], s[40:41] offset:2048\n\t" \
  "global_load_lds_dwordx4 %[wo3], s[40:41] offset:3072\n\t" \
  "s_add_u32 s40, s40, 131072\n\t" \
  "s_addc_u32 s41, s41, 0\n\t" \
  "s_waitcnt vmcnt(24)\n\t" \
  "ds_read_b64_tr_b16 v[80:81], %[lr0] offset:4096\n\t" \
  "ds_read_b64_tr_b16 v[82:83], %[lr0] offset:5120\n\t" \
  "ds_read_b64_tr_b16 v[84:85], %[lr1] offset:4096\n\t" \
  "ds_read_b64_tr_b16 v[86:87], %[lr1] offset:5120\n\t" \
  "ds_read_b64_tr_b16 v[88:89], %[lr0] offset:6144\n\t" \
  "ds_read_b64_tr_b16 v[90:91], %[lr0] offset:7168\n\t" \
  "ds_read_b64_tr_b16 v[92:93], %[lr1] offset:6144\n\t" \
  "ds_read_b64_tr_b16 v[94:95], %[lr1] offset:7168\n\t" \
  "s_setprio 0\n\t" \
  "v_mfma_f32_32x32x16_bf16 v[0:15], v[96:99], v[64:67], v[0:15]\n\t" \
  "v_mfma_f32_32x32x16_bf16 v[32:47], v[100:103], v[64:67], v[32:47]\n\t" \
  "v_mfma_f32_32x32x16_bf16 v[16:31], v[96:99], v[68:71], v[16:31]\n\t" \
  "v_mfma_f32_32x32x16_bf16 v[48:63], v[100:103], v[68:71], v[48:63]\n\t" \
  "v_mfma_f32_32x32x16_bf16 v[0:15], v[104:107], v[72:75], v[0:15]\n\t" \
  "v_mfma_f32_32x32x16_bf16 v[32:47], v[108:111], v[72:75], v[32:47]\n\t" \
  "v_mfma_f32_32x32x16_bf16 v[16:31], v[104:107], v[76:79], v[16:31]\n\t" \
  "v_mfma_f32_32x32x16_bf16 v[48:63], v[108:111], v[76:79], v[48:63]\n\t" \
  "s_setprio 2\n\t" \
  "global_load_dwordx4 v[96:99], %[xo0], s[42:43] offset:0 sc1\n\t" \
  "global_load_dwordx4 v[100:103], %[xo1], s[42:43] offset:0 sc1\n\t" \
  "global_load_dwordx4 v[104:107], %[xo0], s[42:43] offset:1024 sc1\n\t" \
  "global_load_dwordx4 v[108:111], %[xo1], s[42:43] offset:1024 sc1\n\t" \
  "s_add_u32 s42, s42, 2048\n\t" \
  "s_addc_u32 s43, s43, 0\n\t" \
  "s_waitcnt lgkmcnt(0)\n\t" \
  "s_add_u32 m0, %[ldsb], 4096\n\t" \
  "s_nop 0\n\t" \
  "global_load_lds_dwordx4 %[wo0], s[40:41] offset:0\n\t" \
  "global_load_lds_dwordx4 %[wo1], s[40:41] offset:1024\n\t" \
  "global_load_lds_dwordx4 %[wo2], s[40:41] offset:2048\n\t" \
  "global_load_lds_dwordx4 %[wo3], s[40:41] offset:3072\n\t" \
  "s_add_u32 s40, s40, 131072\n\t" \
  "s_addc_u32 s41, s41, 0\n\t" \
  "s_waitcnt vmcnt(24)\n\t" \
  "ds_read_b64_tr_b16 v[64:65], %[lr0] offset:8192\n\t" \
  "ds_read_b64_tr_b16 v[66:67], %[lr0] offset:9216\n\t" \
  "ds_read_b64_tr_b16 v[68:69], %[lr1] offset:8192\n\t" \
  "ds_read_b64_tr_b16 v[70:71], %[lr1] offset:9216\n\t" \
  "ds_read_b64_tr_b16 v[72:73], %[lr0] offset:10240\n\t" \
  "ds_read_b64_tr_b16 v[74:75], %[lr0] offset:11264\n\t" \
  "ds_read_b64_tr_b16 v[76:77], %[lr1] offset:10240\n\t" \
  "ds_read_b64_tr_b16 v[78:79], %[lr1] offset:11264\n\t" \
  "s_setprio 0\n\t" \
  "v_mfma_f32_32x32x16_bf16 v[0:15], v[112:115], v[80:83], v[0:15]\n\t" \
  "v_mfma_f32_32x32x16_bf16 v[32:47], v[116:119], v[80:83], v[32:47]\n\t" \
  "v_mfma_f32_32x32x16_bf16 v[16:31], v[112:115], v[84:87], v[16:31]\n\t" \
  "v_mfma_f32_32x32x16_bf16 v[48:63], v[116:119], v[84:87], v[48:63]\n\t" \
  "v_mfma_f32_32x32x16_bf16 v[0:15], v[120:123], v[88:91], v[0:15]\n\t" \
  "v_mfma_f32_32x32x16_bf16 v[32:47], v[124:127], v[88:91], v[32:47]\n\t" \
  "v_mfma_f32_32x32x16_bf16 v[16:31], v[120:123], v[92:95], v[16:31]\n\t" \
  "v_mfma_f32_32x32x16_bf16 v[48:63], v[124:127], v[92:95], v[48:63]\n\t" \
  "s_setprio 2\n\t" \
  "global_load_dwordx4 v[112:115], %[xo0], s[42:43] offset:0 sc1\n\t" \
  "global_load_dwordx4 v[116:119], %[xo1], s[42:43] offset:0 sc1\n\t" \
  "global_load_dwordx4 v[120:123], %[xo0], s[42:43] offset:1024 sc1\n\t" \
  "global_load_dwordx4 v[124:127], %[xo1], s[42:43] offset:1024 sc1\n\t" \
  "s_add_u32 s42, s42, 2048\n\t" \
  "s_addc_u32 s43, s43, 0\n\t" \
  "s_waitcnt lgkmcnt(0)\n\t" \
  "s_add_u32 m0, %[ldsb], 8192\n\t" \
  "s_nop 0\n\t" \
  "global_load_lds_dwordx4 %[wo0], s[40:41] offset:0\n\t" \
  "global_load_lds_dwordx4 %[wo1], s[40:41] offset:1024\n\t" \
  "global_load_lds_dwordx4 %[wo2], s[40:41] offset:2048\n\t" \
  "global_load_lds_dwordx4 %[wo3], s[40:41] offset:3072\n\t" \
  "s_add_u32 s40, s40, 131072\n\t" \
  "s_addc_u32 s41, s41, 0\n\t" \
  "s_waitcnt vmcnt(24)\n\t" \
  "ds_read_b64_tr_b16 v[80:81], %[lr0] offset:12288\n\t" \
  "ds_read_b64_tr_b16 v[82:83], %[lr0] offset:13312\n\t" \
  "ds_read_b64_tr_b16 v[84:85], %[lr1] offset:12288\n\t" \
  "ds_read_b64_tr_b16 v[86:87], %[lr1] offset:13312\n\t" \
  "ds_read_b64_tr_b16 v[88:89], %[lr0] offset:14336\n\t" \
  "ds_read_b64_tr_b16 v[90:91], %[lr0] offset:15360\n\t" \
  "ds_read_b64_tr_b16 v[92:93], %[lr1] offset:14336\n\t" \
  "ds_read_b64_tr_b16 v[94:95], %[lr1] offset:15360\n\t" \
  "s_setprio 0\n\t" \
  "v_mfma_f32_32x32x16_bf16 v[0:15], v[128:131], v[64:67], v[0:15]\n\t" \
  "v_mfma_f32_32x32x16_bf16 v[32:47], v[132:135], v[64:67], v[32:47]\n\t" \
  "v_mfma_f32_32x32x16_bf16 v[16:31], v[128:131], v[68:71], v[16:31]\n\t" \
  "v_mfma_f32_32x32x16_bf16 v[48:63], v[132:135], v[68:71], v[48:63]\n\t" \
  "v_mfma_f32_32x32x16_bf16 v[0:15], v[136:139], v[72:75], v[0:15]\n\t" \
  "v_mfma_f32_32x32x16_bf16 v[32:47], v[140:143], v[72:75], v[32:47]\n\t" \
  "v_mfma_f32_32x32x16_bf16 v[16:31], v[136:139], v[76:79], v[16:31]\n\t" \
  "v_mfma_f32_32x32x16_bf16 v[48:63], v[140:143], v[76:79], v[48:63]\n\t" \
  "s_setprio 2\n\t" \
  "global_load_dwordx4 v[128:131], %[xo0], s[42:43] offset:0 sc1\n\t" \
  "global_load_dwordx4 v[132:135], %[xo1], s[42:43] offset:0 sc1\n\t" \
  "global_load_dwordx4 v[136:139], %[xo0], s[42:43] offset:1024 sc1\n\t" \
  "global_load_dwordx4 v[140:143], %[xo1], s[42:43] offset:1024 sc1\n\t" \
  "s_add_u32 s42, s42, 2048\n\t" \
  "s_addc_u32 s43, s43, 0\n\t" \
  "s_waitcnt lgkmcnt(0)\n\t" \
  "s_add_u32 m0, %[ldsb], 12288\n\t" \
  "s_nop 0\n\t" \
  "global_load_lds_dwordx4 %[wo0], s[40:41] offset:0\n\t" \
  "global_load_lds_dwordx4 %[wo1], s[40:41] offset:1024\n\t" \
  "global_load_lds_dwordx4 %[wo2], s[40:41] offset:2048\n\t" \
  "global_load_lds_dwordx4 %[wo3], s[40:41] offset:3072\n\t" \
  "s_add_u32 s40, s40, 131072\n\t" \
  "s_addc_u32 s41, s41, 0\n\t" \
  "s_waitcnt vmcnt(24)\n\t" \
  "ds_read_b64_tr_b16 v[64:65], %[lr0] offset:0\n\t" \
  "ds_read_b64_tr_b16 v[66:67], %[lr0] offset:1024\n\t" \
  "ds_read_b64_tr_b16 v[68:69], %[lr1] offset:0\n\t" \
  "ds_read_b64_tr_b16 v[70:71], %[lr1] offset:1024\n\t" \
  "ds_read_b64_tr_b16 v[72:73], %[lr0] offset:2048\n\t" \
  "ds_read_b64_tr_b16 v[74:75], %[lr0] offset:3072\n\t" \
  "ds_read_b64_tr_b16 v[76:77], %[lr1] offset:2048\n\t" \
  "ds_read_b64_tr_b16 v[78:79], %[lr1] offset:3072\n\t" \
  "s_setprio 0\n\t" \
  "v_mfma_f32_32x32x16_bf16 v[0:15], v[144:147], v[80:83], v[0:15]\n\t" \
  "v_mfma_f32_32x32x16_bf16 v[32:47], v[148:151], v[80:83], v[32:47]\n\t" \
  "v_mfma_f32_32x32x16_bf16 v[16:31], v[144:147], v[84:87], v[16:31]\n\t" \
  "v_mfma_f32_32x32x16_bf16 v[48:63], v[148:151], v[84:87], v[48:63]\n\t" \
  "v_mfma_f32_32x32x16_bf16 v[0:15], v[152:155], v[88:91], v[0:15]\n\t" \
  "v_mfma_f32_32x32x16_bf16 v[32:47], v[156:159], v[88:91], v[32:47]\n\t" \
  "v_mfma_f32_32x32x16_bf16 v[16:31], v[152:155], v[92:95], v[16:31]\n\t" \
  "v_mfma_f32_32x32x16_bf16 v[48:63], v[156:159], v[92:95], v[48:63]\n\t" \
  "s_setprio 2\n\t" \
  "global_load_dwordx4 v[144:147], %[xo0], s[42:43] offset:0 sc1\n\t" \
  "global_load_dwordx4 v[148:151], %[xo1], s[42:43] offset:0 sc1\n\t" \
  "global_load_dwordx4 v[152:155], %[xo0], s[42:43] offset:1024 sc1\n\t" \
  "global_load_dwordx4 v[156:159], %[xo1], s[42:43] offset:1024 sc1\n\t" \
  "s_add_u32 s42, s42, 2048\n\t" \
  "s_addc_u32 s43, s43, 0\n\t" \
  "s_sub_u32 %[cnt], %[cnt], 1\n\t" \
  "s_cmp_lg_u32 %[cnt], 0\n\t" \
  "s_cbranch_scc1 2b\n\t" \
  "s_waitcnt lgkmcnt(0)\n\t" \
  "s_add_u32 m0, %[ldsb], 0\n\t" \
  "s_nop 0\n\t" \
  "global_load_lds_dwordx4 %[wo0], s[40:41] offset:0\n\t" \
  "global_load_lds_dwordx4 %[wo1], s[40:41] offset:1024\n\t" \
  "global_load_lds_dwordx4 %[wo2], s[40:41] offset:2048\n\t" \
  "global_load_lds_dwordx4 %[wo3], s[40:41] offset:3072\n\t" \
  "s_add_u32 s40, s40, 131072\n\t" \
  "s_addc_u32 s41, s41, 0\n\t" \
  "s_waitcnt vmcnt(24)\n\t" \
  "ds_read_b64_tr_b16 v[80:81], %[lr0] offset:4096\n\t" \
  "ds_read_b64_tr_b16 v[82:83], %[lr0] offset:5120\n\t" \
  "ds_read_b64_tr_b16 v[84:85], %[lr1] offset:4096\n\t" \
  "ds_read_b64_tr_b16 v[86:87], %[lr1] offset:5120\n\t" \
  "ds_read_b64_tr_b16 v[88:89], %[lr0] offset:6144\n\t" \
  "ds_read_b64_tr_b16 v[90:91], %[lr0] offset:7168\n\t" \
  "ds_read_b64_tr_b16 v[92:93], %[lr1] offset:6144\n\t" \
  "ds_read_b64_tr_b16 v[94:95], %[lr1] offset:7168\n\t" \
  "s_setprio 0\n\t" \
  "v_mfma_f32_32x32x16_bf16 v[0:15], v[96:99], v[64:67], v[0:15]\n\t" \
  "v_mfma_f32_32x32x16_bf16 v[32:47], v[100:103], v[64:67], v[32:47]\n\t" \
  "v_mfma_f32_32x32x16_bf16 v[16:31], v[96:99], v[68:71], v[16:31]\n\t" \
  "v_mfma_f32_32x32x16_bf16 v[48:63], v[100:103], v[68:71], v[48:63]\n\t" \
  "v_mfma_f32_32x32x16_bf16 v[0:15], v[104:107], v[72:75], v[0:15]\n\t" \
  "v_mfma_f32_32x32x16_bf16 v[32:47], v[108:111], v[72:75], v[32:47]\n\t" \
  "v_mfma_f32_32x32x16_bf16 v[16:31], v[104:107], v[76:79], v[16:31]\n\t" \
  "v_mfma_f32_32x32x16_bf16 v[48:63], v[108:111], v[76:79], v[48:63]\n\t" \
  "s_setprio 2\n\t" \
  "global_load_dwordx4 v[96:99], %[xo0], s[42:43] offset:0 sc1\n\t" \
  "global_load_dwordx4 v[100:103], %[xo1], s[42:43] offset:0 sc1\n\t" \
  "global_load_dwordx4 v[104:107], %[xo0], s[42:43] offset:1024 sc1\n\t" \
  "global_load_dwordx4 v[108:111], %[xo1], s[42:43] offset:1024 sc1\n\t" \
  "s_add_u32 s42, s42, 2048\n\t" \
  "s_addc_u32 s43, s43, 0\n\t" \
  "s_waitcnt lgkmcnt(0)\n\t" \
  "s_add_u32 m0, %[ldsb], 4096\n\t" \
  "s_nop 0\n\t" \
  "global_load_lds_dwordx4 %[wo0], s[40:41] offset:0\n\t" \
  "global_load_lds_dwordx4 %[wo1], s[40:41] offset:1024\n\t" \
  "global_load_lds_dwordx4 %[wo2], s[40:41] offset:2048\n\t" \
  "global_load_lds_dwordx4 %[wo3], s[40:41] offset:3072\n\t" \
  "s_add_u32 s40, s40, 131072\n\t" \
  "s_addc_u32 s41, s41, 0\n\t" \
  "s_waitcnt vmcnt(24)\n\t" \
  "ds_read_b64_tr_b16 v[64:65], %[lr0] offset:8192\n\t" \
  "ds_read_b64_tr_b16 v[66:67], %[lr0] offset:9216\n\t" \
  "ds_read_b64_tr_b16 v[68:69], %[lr1] offset:8192\n\t" \
  "ds_read_b64_tr_b16 v[70:71], %[lr1] offset:9216\n\t" \
  "ds_read_b64_tr_b16 v[72:73], %[lr0] offset:10240\n\t" \
  "ds_read_b64_tr_b16 v[74:75], %[lr0] offset:11264\n\t" \
  "ds_read_b64_tr_b16 v[76:77], %[lr1] offset:10240\n\t" \
  "ds_read_b64_tr_b16 v[78:79], %[lr1] offset:11264\n\t" \
  "s_setprio 0\n\t" \
  "v_mfma_f32_32x32x16_bf16 v[0:15], v[112:115], v[80:83], v[0:15]\n\t" \
  "v_mfma_f32_32x32x16_bf16 v[32:47], v[116:119], v[80:83], v[32:47]\n\t" \
  "v_mfma_f32_32x32x16_bf16 v[16:31], v[112:115], v[84:87], v[16:31]\n\t" \
  "v_mfma_f32_32x32x16_bf16 v[48:63], v[116:119], v[84:87], v[48:63]\n\t" \
  "v_mfma_f32_32x32x16_bf16 v[0:15], v[120:123], v[88:91], v[0:15]\n\t" \
  "v_mfma_f32_32x32x16_bf16 v[32:47], v[124:127], v[88:91], v[32:47]\n\t" \
  "v_mfma_f32_32x32x16_bf16 v[16:31], v[120:123], v[92:95], v[16:31]\n\t" \
  "v_mfma_f32_32x32x16_bf16 v[48:63], v[124:127], v[92:95], v[48:63]\n\t" \
  "s_setprio 2\n\t" \
  "global_load_dwordx4 v[112:115], %[xo0], s[42:43] offset:0 sc1\n\t" \
  "global_load_dwordx4 v[116:119], %[xo1], s[42:43] offset:0 sc1\n\t" \
  "global_load_dwordx4 v[120:123], %[xo0], s[42:43] offset:1024 sc1\n\t" \
  "global_load_dwordx4 v[124:127], %[xo1], s[42:43] offset:1024 sc1\n\t" \
  "s_add_u32 s42, s42, 2048\n\t" \
  "s_addc_u32 s43, s43, 0\n\t" \
  "s_waitcnt lgkmcnt(0)\n\t" \
  "s_waitcnt vmcnt(20)\n\t" \
  "ds_read_b64_tr_b16 v[80:81], %[lr0] offset:12288\n\t" \
  "ds_read_b64_tr_b16 v[82:83], %[lr0] offset:13312\n\t" \
  "ds_read_b64_tr_b16 v[84:85], %[lr1] offset:12288\n\t" \
  "ds_read_b64_tr_b16 v[86:87], %[lr1] offset:13312\n\t" \
  "ds_read_b64_tr_b16 v[88:89], %[lr0] offset:14336\n\t" \
  "ds_read_b64_tr_b16 v[90:91], %[lr0] offset:15360\n\t" \
  "ds_read_b64_tr_b16 v[92:93], %[lr1] offset:14336\n\t" \
  "ds_read_b64_tr_b16 v[94:95], %[lr1] offset:15360\n\t" \
  "s_setprio 0\n\t" \
  "v_mfma_f32_32x32x16_bf16 v[0:15], v[128:131], v[64:67], v[0:15]\n\t" \
  "v_mfma_f32_32x32x16_bf16 v[32:47], v[132:135], v[64:67], v[32:47]\n\t" \
  "v_mfma_f32_32x32x16_bf16 v[16:31], v[128:131], v[68:71], v[16:31]\n\t" \
  "v_mfma_f32_32x32x16_bf16 v[48:63], v[132:135], v[68:71], v[48:63]\n\t" \
  "v_mfma_f32_32x32x16_bf16 v[0:15], v[136:139], v[72:75], v[0:15]\n\t" \
  "v_mfma_f32_32x32x16_bf16 v[32:47], v[140:143], v[72:75], v[32:47]\n\t" \
  "v_mfma_f32_32x32x16_bf16 v[16:31], v[136:139], v[76:79], v[16:31]\n\t" \
  "v_mfma_f32_32x32x16_bf16 v[48:63], v[140:143], v[76:79], v[48:63]\n\t" \
  "s_setprio 2\n\t" \
  "s_waitcnt lgkmcnt(0)\n\t" \
  "s_waitcnt vmcnt(12)\n\t" \
  "ds_read_b64_tr_b16 v[64:65], %[lr0] offset:0\n\t" \
  "ds_read_b64_tr_b16 v[66:67], %[lr0] offset:1024\n\t" \
  "ds_read_b64_tr_b16 v[68:69], %[lr1] offset:0\n\t" \
  "ds_read_b64_tr_b16 v[70:71], %[lr1] offset:1024\n\t" \
  "ds_read_b64_tr_b16 v[72:73], %[lr0] offset:2048\n\t" \
  "ds_read_b64_tr_b16 v[74:75], %[lr0] offset:3072\n\t" \
  "ds_read_b64_tr_b16 v[76:77], %[lr1] offset:2048\n\t" \
  "ds_read_b64_tr_b16 v[78:79], %[lr1] offset:3072\n\t" \
  "s_setprio 0\n\t" \
  "v_mfma_f32_32x32x16_bf16 v[0:15], v[144:147], v[80:83], v[0:15]\n\t" \
  "v_mfma_f32_32x32x16_bf16 v[32:47], v[148:151], v[80:83], v[32:47]\n\t" \
  "v_mfma_f32_32x32x16_bf16 v[16:31], v[144:147], v[84:87], v[16:31]\n\t" \
  "v_mfma_f32_32x32x16_bf16 v[48:63], v[148:151], v[84:87], v[48:63]\n\t" \
  "v_mfma_f32_32x32x16_bf16 v[0:15], v[152:155], v[88:91], v[0:15]\n\t" \
  "v_mfma_f32_32x32x16_bf16 v[32:47], v[156:159], v[88:91], v[32:47]\n\t" \
  "v_mfma_f32_32x32x16_bf16 v[16:31], v[152:155], v[92:95], v[16:31]\n\t" \
  "v_mfma_f32_32x32x16_bf16 v[48:63], v[156:159], v[92:95], v[48:63]\n\t" \
  "s_setprio 2\n\t" \
  "s_waitcnt lgkmcnt(0)\n\t" \
  "s_waitcnt vmcnt(4)\n\t" \
  "ds_read_b64_tr_b16 v[80:81], %[lr0] offset:4096\n\t" \
  "ds_read_b64_tr_b16 v[82:83], %[lr0] offset:5120\n\t" \
  "ds_read_b64_tr_b16 v[84:85], %[lr1] offset:4096\n\t" \
  "ds_read_b64_tr_b16 v[86:87], %[lr1] offset:5120\n\t" \
  "ds_read_b64_tr_b16 v[88:89], %[lr0] offset:6144\n\t" \
  "ds_read_b64_tr_b16 v[90:91], %[lr0] offset:7168\n\t" \
  "ds_read_b64_tr_b16 v[92:93], %[lr1] offset:6144\n\t" \
  "ds_read_b64_tr_b16 v[94:95], %[lr1] offset:7168\n\t" \
  "s_setprio 0\n\t" \
  "v_mfma_f32_32x32x16_bf16 v[0:15], v[96:99], v[64:67], v[0:15]\n\t" \
  "v_mfma_f32_32x32x16_bf16 v[32:47], v[100:103], v[64:67], v[32:47]\n\t" \
  "v_mfma_f32_32x32x16_bf16 v[16:31], v[96:99], v[68:71], v[16:31]\n\t" \
  "v_mfma_f32_32x32x16_bf16 v[48:63], v[100:103], v[68:71], v[48:63]\n\t" \
  "v_mfma_f32_32x32x16_bf16 v[0:15], v[104:107], v[72:75], v[0:15]\n\t" \
  "v_mfma_f32_32x32x16_bf16 v[32:47], v[108:111], v[72:75], v[32:47]\n\t" \
  "v_mfma_f32_32x32x16_bf16 v[16:31], v[104:107], v[76:79], v[16:31]\n\t" \
  "v_mfma_f32_32x32x16_bf16 v[48:63], v[108:111], v[76:79], v[48:63]\n\t" \
  "s_setprio 2\n\t" \
  "s_waitcnt lgkmcnt(0)\n\t" \
  "s_waitcnt vmcnt(0)\n\t" \
  "s_setprio 0\n\t" \
  "v_mfma_f32_32x32x16_bf16 v[0:15], v[112:115], v[80:83], v[0:15]\n\t" \
  "v_mfma_f32_32x32x16_bf16 v[32:47], v[116:119], v[80:83], v[32:47]\n\t" \
  "v_mfma_f32_32x32x16_bf16 v[16:31], v[112:115], v[84:87], v[16:31]\n\t" \
  "v_mfma_f32_32x32x16_bf16 v[48:63], v[116:119], v[84:87], v[48:63]\n\t" \
  "v_mfma_f32_32x32x16_bf16 v[0:15], v[120:123], v[88:91], v[0:15]\n\t" \
  "v_mfma_f32_32x32x16_bf16 v[32:47], v[124:127], v[88:91], v[32:47]\n\t" \
  "v_mfma_f32_32x32x16_bf16 v[16:31], v[120:123], v[92:95], v[16:31]\n\t" \
  "v_mfma_f32_32x32x16_bf16 v[48:63], v[124:127], v[92:95], v[48:63]\n\t" \
  "s_setprio 2\n\t" \
  "s_mov_b32 m0, %[m0s]\n\t" \
  "s_nop 7\n\t" \
  "s_nop 3\n\t" \
  ""
#define G2_CLOB "v64", "v65", "v66", "v67", "v68", "v69", "v70", "v71", "v72", "v73", "v74", "v75", "v76", "v77", "v78", "v79", "v80", "v81", "v82", "v83", "v84", "v85", "v86", "v87", "v88", "v89", "v90", "v91", "v92", "v93", "v94", "v95", "v96", "v97", "v98", "v99", "v100", "v101", "v102", "v103", "v104", "v105", "v106", "v107", "v108", "v109", "v110", "v111", "v112", "v113", "v114", "v115", "v116", "v117", "v118", "v119", "v120", "v121", "v122", "v123", "v124", "v125", "v126", "v127", "v128", "v129", "v130", "v131", "v132", "v133", "v134", "v135", "v136", "v137", "v138", "v139", "v140", "v141", "v142", "v143", "v144", "v145", "v146", "v147", "v148", "v149", "v150", "v151", "v152", "v153", "v154", "v155", "v156", "v157", "v158", "v159"
#define G3P_ASM \
  "s_mov_b32 %[m0s], m0\n\t" \
  "s_add_u32 m0, %[ldsb], 0\n\t" \
  "s_nop 0\n\t" \
  "global_load_lds_dwordx4 %[wo0], s[40:41] offset:0\n\t" \
  "global_load_lds_dwordx4 %[wo1], s[40:41] offset:1024\n\t" \
  "global_load_lds_dwordx4 %[wo2], s[40:41] offset:2048\n\t" \
  "global_load_lds_dwordx4 %[wo3], s[40:41] offset:3072\n\t" \
  "s_add_u32 s40, s40, 32768\n\t" \
  "s_addc_u32 s41, s41, 0\n\t" \
  "s_add_u32 m0, %[ldsb], 4096\n\t" \
  "s_nop 0\n\t" \
  "global_load_lds_dwordx4 %[wo0], s[40:41] offset:0\n\t" \
  "global_load_lds_dwordx4 %[wo1], s[40:41] offset:1024\n\t" \
  "global_load_lds_dwordx4 %[wo2], s[40:41] offset:2048\n\t" \
  "global_load_lds_dwordx4 %[wo3], s[40:41] offset:3072\n\t" \
  "s_add_u32 s40, s40, 32768\n\t" \
  "s_addc_u32 s41, s41, 0\n\t" \
  "s_add_u32 m0, %[ldsb], 8192\n\t" \
  "s_nop 0\n\t" \
  "global_load_lds_dwordx4 %[wo0], s[40:41] offset:0\n\t" \
  "global_load_lds_dwordx4 %[wo1], s[40:41] offset:1024\n\t" \
  "global_load_lds_dwordx4 %[wo2], s[40:41] offset:2048\n\t" \
  "global_load_lds_dwordx4 %[wo3], s[40:41] offset:3072\n\t" \
  "s_add_u32 s40, s40, 32768\n\t" \
  "s_addc_u32 s41, s41, 0\n\t" \
  "s_add_u32 m0, %[ldsb], 12288\n\t" \
  "s_nop 0\n\t" \
  "global_load_lds_dwordx4 %[wo0], s[40:41] offset:0\n\t" \
  "global_load_lds_dwordx4 %[wo1], s[40:41] offset:1024\n\t" \
  "global_load_lds_dwordx4 %[wo2], s[40:41] offset:2048\n\t" \
  "global_load_lds_dwordx4 %[wo3], s[40:41] offset:3072\n\t" \
  "s_add_u32 s40, s40, 32768\n\t" \
  "s_addc_u32 s41, s41, 0\n\t" \
  "s_mov_b32 m0, %[m0s]\n\t" \
  ""
#define G3_ASM \
  "s_mov_b32 %[m0s], m0\n\t" \
  "global_load_dwordx4 v[96:99], %[xo0], s[42:43] offset:0 sc1\n\t" \
  "global_load_dwordx4 v[100:103], %[xo1], s[42:43] offset:0 sc1\n\t" \
  "global_load_dwordx4 v[104:107], %[xo0], s[42:43] offset:1024 sc1\n\t" \
  "global_load_dwordx4 v[108:111], %[xo1], s[42:43] offset:1024 sc1\n\t" \
  "s_add_u32 s42, s42, 2048\n\t" \
  "s_addc_u32 s43, s43, 0\n\t" \
  "global_load_dwordx4 v[112:115], %[xo0], s[42:43] offset:0 sc1\n\t" \
  "global_load_dwordx4 v[116:119], %[xo1], s[42:43] offset:0 sc1\n\t" \
  "global_load_dwordx4 v[120:123], %[xo0], s[42:43] offset:1024 sc1\n\t" \
  "global_load_dwordx4 v[124:127], %[xo1], s[42:43] offset:1024 sc1\n\t" \
  "s_add_u32 s42, s42, 2048\n\t" \
  "s_addc_u32 s43, s43, 0\n\t" \
  "global_load_dwordx4 v[128:131], %[xo0], s[42:43] offset:0 sc1\n\t" \
  "global_load_dwordx4 v[132:135], %[xo1], s[42:43] offset:0 sc1\n\t" \
  "global_load_dwordx4 v[136:139], %[xo0], s[42:43] offset:1024 sc1\n\t" \
  "global_load_dwordx4 v[140:143], %[xo1], s[42:43] offset:1024 sc1\n\t" \
  "s_add_u32 s42, s42, 2048\n\t" \
  "s_addc_u32 s43, s43, 0\n\t" \
  "global_load_dwordx4 v[144:147], %[xo0], s[42:43] offset:0 sc1\n\t" \
  "global_load_dwordx4 v[148:151], %[xo1], s[42:43] offset:0 sc1\n\t" \
  "global_load_dwordx4 v[152:155], %[xo0], s[42:43] offset:1024 sc1\n\t" \
  "global_load_dwordx4 v[156:159], %[xo1], s[42:43] offset:1024 sc1\n\t" \
  "s_add_u32 s42, s42, 2048\n\t" \
  "s_addc_u32 s43, s43, 0\n\t" \
  "v_mov_b32 v80, 0\n\t" \
  "v_mov_b32 v81, 0\n\t" \
  "v_mov_b32 v82, 0\n\t" \
  "v_mov_b32 v83, 0\n\t" \
  "s_nop 1\n\t" \
  "v_mfma_f32_32x32x16_bf16 v[0:15], v[80:83], v[80:83], 0\n\t" \
  "v_mfma_f32_32x32x16_bf16 v[16:31], v[80:83], v[80:83], 0\n\t" \
  "v_mfma_f32_32x32x16_bf16 v[32:47], v[80:83], v[80:83], 0\n\t" \
  "v_mfma_f32_32x32x16_bf16 v[48:63], v[80:83], v[80:83], 0\n\t" \
  "s_waitcnt vmcnt(28)\n\t" \
  "ds_read_b64_tr_b16 v[64:65], %[lr0] offset:0\n\t" \
  "ds_read_b64_tr_b16 v[66:67], %[lr0] offset:1024\n\t" \
  "ds_read_b64_tr_b16 v[68:69], %[lr1] offset:0\n\t" \
  "ds_read_b64_tr_b16 v[70:71], %[lr1] offset:1024\n\t" \
  "ds_read_b64_tr_b16 v[72:73], %[lr0] offset:2048\n\t" \
  "ds_read_b64_tr_b16 v[74:75], %[lr0] offset:3072\n\t" \
  "ds_read_b64_tr_b16 v[76:77], %[lr1] offset:2048\n\t" \
  "ds_read_b64_tr_b16 v[78:79], %[lr1] offset:3072\n\t" \
  "s_waitcnt lgkmcnt(0)\n\t" \
  "s_waitcnt vmcnt(12)\n\t" \
  "ds_read_b64_tr_b16 v[80:81], %[lr0] offset:4096\n\t" \
  "ds_read_b64_tr_b16 v[82:83], %[lr0] offset:5120\n\t" \
  "ds_read_b64_tr_b16 v[84:85], %[lr1] offset:4096\n\t" \
  "ds_read_b64_tr_b16 v[86:87], %[lr1] offset:5120\n\t" \
  "ds_read_b64_tr_b16 v[88:89], %[lr0] offset:6144\n\t" \
  "ds_read_b64_tr_b16 v[90:91], %[lr0] offset:7168\n\t" \
  "ds_read_b64_tr_b16 v[92:93], %[lr1] offset:6144\n\t" \
  "ds_read_b64_tr_b16 v[94:95], %[lr1] offset:7168\n\t" \
  "v_mfma_f32_32x32x16_bf16 v[0:15], v[64:67], v[96:99], v[0:15]\n\t" \
  "v_mfma_f32_32x32x16_bf16 v[16:31], v[64:67], v[100:103], v[16:31]\n\t" \
  "v_mfma_f32_32x32x16_bf16 v[32:47], v[68:71], v[96:99], v[32:47]\n\t" \
  "v_mfma_f32_32x32x16_bf16 v[48:63], v[68:71], v[100:103], v[48:63]\n\t" \
  "v_mfma_f32_32x32x16_bf16 v[0:15], v[72:75], v[104:107], v[0:15]\n\t" \
  "v_mfma_f32_32x32x16_bf16 v[16:31], v[72:75], v[108:111], v[16:31]\n\t" \
  "v_mfma_f32_32x32x16_bf16 v[32:47], v[76:79], v[104:107], v[32:47]\n\t" \
  "v_mfma_f32_32x32x16_bf16 v[48:63], v[76:79], v[108:111], v[48:63]\n\t" \
  "s_waitcnt lgkmcnt(0)\n\t" \
  "s_waitcnt vmcnt(8)\n\t" \
  "ds_read_b64_tr_b16 v[64:65], %[lr0] offset:8192\n\t" \
  "ds_read_b64_tr_b16 v[66:67], %[lr0] offset:9216\n\t" \
  "ds_read_b64_tr_b16 v[68:69], %[lr1] offset:8192\n\t" \
  "ds_read_b64_tr_b16 v[70:71], %[lr1] offset:9216\n\t" \
  "ds_read_b64_tr_b16 v[72:73], %[lr0] offset:10240\n\t" \
  "ds_read_b64_tr_b16 v[74:75], %[lr0] offset:11264\n\t" \
  "ds_read_b64_tr_b16 v[76:77], %[lr1] offset:10240\n\t" \
  "ds_read_b64_tr_b16 v[78:79], %[lr1] offset:11264\n\t" \
  "v_mfma_f32_32x32x16_bf16 v[0:15], v[80:83], v[112:115], v[0:15]\n\t" \
  "v_mfma_f32_32x32x16_bf16 v[16:31], v[80:83], v[116:119], v[16:31]\n\t" \
  "v_mfma_f32_32x32x16_bf16 v[32:47], v[84:87], v[112:115], v[32:47]\n\t" \
  "v_mfma_f32_32x32x16_bf16 v[48:63], v[84:87], v[116:119], v[48:63]\n\t" \
  "v_mfma_f32_32x32x16_bf16 v[0:15], v[88:91], v[120:123], v[0:15]\n\t" \
  "v_mfma_f32_32x32x16_bf16 v[16:31], v[88:91], v[124:127], v[16:31]\n\t" \
  "v_mfma_f32_32x32x16_bf16 v[32:47], v[92:95], v[120:123], v[32:47]\n\t" \
  "v_mfma_f32_32x32x16_bf16 v[48:63], v[92:95], v[124:127], v[48:63]\n\t" \
  "s_waitcnt lgkmcnt(0)\n\t" \
  "s_waitcnt vmcnt(4)\n\t" \
  "ds_read_b64_tr_b16 v[80:81], %[lr0] offset:12288\n\t" \
  "ds_read_b64_tr_b16 v[82:83], %[lr0] offset:13312\n\t" \
  "ds_read_b64_tr_b16 v[84:85], %[lr1] offset:12288\n\t" \
  "ds_read_b64_tr_b16 v[86:87], %[lr1] offset:13312\n\t" \
  "ds_read_b64_tr_b16 v[88:89], %[lr0] offset:14336\n\t" \
  "ds_read_b64_tr_b16 v[90:91], %[lr0] offset:15360\n\t" \
  "ds_read_b64_tr_b16 v[92:93], %[lr1] offset:14336\n\t" \
  "ds_read_b64_tr_b16 v[94:95], %[lr1] offset:15360\n\t" \
  "v_mfma_f32_32x32x16_bf16 v[0:15], v[64:67], v[128:131], v[0:15]\n\t" \
  "v_mfma_f32_32x32x16_bf16 v[16:31], v[64:67], v[132:135], v[16:31]\n\t" \
  "v_mfma_f32_32x32x16_bf16 v[32:47], v[68:71], v[128:131], v[32:47]\n\t" \
  "v_mfma_f32_32x32x16_bf16 v[48:63], v[68:71], v[132:135], v[48:63]\n\t" \
  "v_mfma_f32_32x32x16_bf16 v[0:15], v[72:75], v[136:139], v[0:15]\n\t" \
  "v_mfma_f32_32x32x16_bf16 v[16:31], v[72:75], v[140:143], v[16:31]\n\t" \
  "v_mfma_f32_32x32x16_bf16 v[32:47], v[76:79], v[136:139], v[32:47]\n\t" \
  "v_mfma_f32_32x32x16_bf16 v[48:63], v[76:79], v[140:143], v[48:63]\n\t" \
  "s_waitcnt lgkmcnt(0)\n\t" \
  "s_waitcnt vmcnt(0)\n\t" \
  "v_mfma_f32_32x32x16_bf16 v[0:15], v[80:83], v[144:147], v[0:15]\n\t" \
  "v_mfma_f32_32x32x16_bf16 v[16:31], v[80:83], v[148:151], v[16:31]\n\t" \
  "v_mfma_f32_32x32x16_bf16 v[32:47], v[84:87], v[144:147], v[32:47]\n\t" \
  "v_mfma_f32_32x32x16_bf16 v[48:63], v[84:87], v[148:151], v[48:63]\n\t" \
  "v_mfma_f32_32x32x16_bf16 v[0:15], v[88:91], v[152:155], v[0:15]\n\t" \
  "v_mfma_f32_32x32x16_bf16 v[16:31], v[88:91], v[156:159], v[16:31]\n\t" \
  "v_mfma_f32_32x32x16_bf16 v[32:47], v[92:95], v[152:155], v[32:47]\n\t" \
  "v_mfma_f32_32x32x16_bf16 v[48:63], v[92:95], v[156:159], v[48:63]\n\t" \
  "s_mov_b32 m0, %[m0s]\n\t" \
  "s_nop 7\n\t" \
  "s_nop 3\n\t" \
  ""
#define G3_CLOB "v64", "v65", "v66", "v67", "v68", "v69", "v70", "v71", "v72", "v73", "v74", "v75", "v76", "v77", "v78", "v79", "v80", "v81", "v82", "v83", "v84", "v85", "v86", "v87", "v88", "v89", "v90", "v91", "v92", "v93", "v94", "v95", "v96", "v97", "v98", "v99", "v100", "v101", "v102", "v103", "v104", "v105", "v106", "v107", "v108", "v109", "v110", "v111", "v112", "v113", "v114", "v115", "v116", "v117", "v118", "v119", "v120", "v121", "v122", "v123", "v124", "v125", "v126", "v127", "v128", "v129", "v130", "v131", "v132", "v133", "v134", "v135", "v136", "v137", "v138", "v139", "v140", "v141", "v142", "v143", "v144", "v145", "v146", "v147", "v148", "v149", "v150", "v151", "v152", "v153", "v154", "v155", "v156", "v157", "v158", "v159"
#define PAP_ASM \
  "s_mov_b32 %[m0s], m0\n\t" \
  "s_mov_b32 %[cnt], 4\n\t" \
  "1:\n\t" \
  "s_add_u32 m0, %[dst], %[xrel]\n\t" \
  "s_nop 0\n\t" \
  "global_load_lds_dwordx4 %[vo0], s[40:41] offset:0\n\t" \
  "global_load_lds_dwordx4 %[vo1], s[40:41] offset:1024\n\t" \
  "s_add_u32 s40, s40, 128\n\t" \
  "s_addc_u32 s41, s41, 0\n\t" \
  "s_add_u32 %[dst], %[dst], 28672\n\t" \
  "s_sub_u32 %[cnt], %[cnt], 1\n\t" \
  "s_cmp_lg_u32 %[cnt], 0\n\t" \
  "s_cbranch_scc1 1b\n\t" \
  "s_mov_b32 m0, %[m0s]\n\t" \
  ""
#define PBP_ASM \
  "s_mov_b32 %[m0s], m0\n\t" \
  "s_mov_b32 %[cnt], 4\n\t" \
  "1:\n\t" \
  "s_add_u32 m0, %[dst], %[xrel]\n\t" \
  "s_nop 0\n\t" \
  "global_load_lds_dwordx4 %[vo0], s[40:41] offset:0\n\t" \
  "global_load_lds_dwordx4 %[vo1], s[40:41] offset:1024\n\t" \
  "s_add_u32 m0, %[dst], %[wrel]\n\t" \
  "s_nop 0\n\t" \
  "global_load_lds_dwordx4 %[vo2], s[42:43] offset:0\n\t" \
  "global_load_lds_dwordx4 %[vo3], s[42:43] offset:1024\n\t" \
  "global_load_lds_dwordx4 %[vo4], s[42:43] offset:2048\n\t" \
  "s_add_u32 s42, s42, 196608\n\t" \
  "s_addc_u32 s43, s43, 0\n\t" \
  "s_add_u32 s40, s40, 128\n\t" \
  "s_addc_u32 s41, s41, 0\n\t" \
  "s_add_u32 %[dst], %[dst], 28672\n\t" \
  "s_sub_u32 %[cnt], %[cnt], 1\n\t" \
  "s_cmp_lg_u32 %[cnt], 0\n\t" \
  "s_cbranch_scc1 1b\n\t" \
  "s_mov_b32 m0, %[m0s]\n\t" \
  ""
#define PAN_ASM \
  "s_mov_b32 %[m0s], m0\n\t" \
  "v_mov_b32 v80, 0\n\t" \
  "v_mov_b32 v81, 0\n\t" \
  "v_mov_b32 v82, 0\n\t" \
  "v_mov_b32 v83, 0\n\t" \
  "s_nop 1\n\t" \
  "v_mfma_f32_32x32x16_bf16 v[0:15], v[80:83], v[80:83], 0\n\t" \
  "v_mfma_f32_32x32x16_bf16 v[16:31], v[80:83], v[80:83], 0\n\t" \
  "s_waitcnt vmcnt(6)\n\t" \
  "s_barrier\n\t" \
  "s_mov_b32 %[s], 0\n\t" \
  "s_mov_b32 %[sl], 0\n\t" \
  "ds_read_b128 v[32:35], %[xa0]\n\t" \
  "ds_read_b64_tr_b16 v[48:49], %[wa] offset:0\n\t" \
  "ds_read_b64_tr_b16 v[50:51], %[wa] offset:768\n\t" \
  "ds_read_b64_tr_b16 v[52:53], %[wa] offset:64\n\t" \
  "ds_read_b64_tr_b16 v[54:55], %[wa] offset:832\n\t" \
  "ds_read_b128 v[36:39], %[xa1]\n\t" \
  "ds_read_b64_tr_b16 v[56:57], %[wa] offset:3072\n\t" \
  "ds_read_b64_tr_b16 v[58:59], %[wa] offset:3840\n\t" \
  "ds_read_b64_tr_b16 v[60:61], %[wa] offset:3136\n\t" \
  "ds_read_b64_tr_b16 v[62:63], %[wa] offset:3904\n\t" \
  "ds_read_b128 v[40:43], %[xa2]\n\t" \
  "ds_read_b64_tr_b16 v[64:65], %[wa] offset:6144\n\t" \
  "ds_read_b64_tr_b16 v[66:67], %[wa] offset:6912\n\t" \
  "ds_read_b64_tr_b16 v[68:69], %[wa] offset:6208\n\t" \
  "ds_read_b64_tr_b16 v[70:71], %[wa] offset:6976\n\t" \
  "ds_read_b128 v[44:47], %[xa3]\n\t" \
  "ds_read_b64_tr_b16 v[72:73], %[wa] offset:9216\n\t" \
  "ds_read_b64_tr_b16 v[74:75], %[wa] offset:9984\n\t" \
  "ds_read_b64_tr_b16 v[76:77], %[wa] offset:9280\n\t" \
  "ds_read_b64_tr_b16 v[78:79], %[wa] offset:10048\n\t" \
  "s_mov_b32 %[dl], 28672\n\t" \
  "s_add_u32 %[sl], %[sl], 1\n\t" \
  "s_cmp_eq_u32 %[sl], 5\n\t" \
  "s_cselect_b32 %[dl], 4294852608, %[dl]\n\t" \
  "s_cselect_b32 %[sl], 0, %[sl]\n\t" \
  "v_add_u32 %[xa0], %[dl], %[xa0]\n\t" \
  "v_add_u32 %[xa1], %[dl], %[xa1]\n\t" \
  "v_add_u32 %[xa2], %[dl], %[xa2]\n\t" \
  "v_add_u32 %[xa3], %[dl], %[xa3]\n\t" \
  "v_add_u32 %[wa], %[dl], %[wa]\n\t" \
  "2:\n\t" \
  "s_cmp_lt_u32 %[s], 5\n\t" \
  "s_cbranch_scc1 3f\n\t" \
  "s_cmp_eq_u32 %[s], 5\n\t" \
  "s_cbranch_scc1 5f\n\t" \
  "s_waitcnt vmcnt(0) lgkmcnt(0)\n\t" \
  "s_branch 6f\n\t" \
  "3:\n\t" \
  "s_waitcnt vmcnt(4) lgkmcnt(0)\n\t" \
  "s_branch 6f\n\t" \
  "5:\n\t" \
  "s_waitcnt vmcnt(2) lgkmcnt(0)\n\t" \
  "6:\n\t" \
  "s_barrier\n\t" \
  "v_mfma_f32_32x32x16_bf16 v[0:15], v[48:51], v[32:35], v[0:15]\n\t" \
  "ds_read_b128 v[80:83], %[xa0]\n\t" \
  "ds_read_b64_tr_b16 v[96:97], %[wa] offset:0\n\t" \
  "ds_read_b64_tr_b16 v[98:99], %[wa] offset:768\n\t" \
  "v_mfma_f32_32x32x16_bf16 v[16:31], v[52:55], v[32:35], v[16:31]\n\t" \
  "ds_read_b64_tr_b16 v[100:101], %[wa] offset:64\n\t" \
  "ds_read_b64_tr_b16 v[102:103], %[wa] offset:832\n\t" \
  "ds_read_b128 v[84:87], %[xa1]\n\t" \
  "v_mfma_f32_32x32x16_bf16 v[0:15], v[56:59], v[36:39], v[0:15]\n\t" \
  "ds_read_b64_tr_b16 v[104:105], %[wa] offset:3072\n\t" \
  "ds_read_b64_tr_b16 v[106:107], %[wa] offset:3840\n\t" \
  "ds_read_b64_tr_b16 v[108:109], %[wa] offset:3136\n\t" \
  "v_mfma_f32_32x32x16_bf16 v[16:31], v[60:63], v[36:39], v[16:31]\n\t" \
  "ds_read_b64_tr_b16 v[110:111], %[wa] offset:3904\n\t" \
  "ds_read_b128 v[88:91], %[xa2]\n\t" \
  "ds_read_b64_tr_b16 v[112:113], %[wa] offset:6144\n\t" \
  "v_mfma_f32_32x32x16_bf16 v[0:15], v[64:67], v[40:43], v[0:15]\n\t" \
  "ds_read_b64_tr_b16 v[114:115], %[wa] offset:6912\n\t" \
  "ds_read_b64_tr_b16 v[116:117], %[wa] offset:6208\n\t" \
  "ds_read_b64_tr_b16 v[118:119], %[wa] offset:6976\n\t" \
  "v_mfma_f32_32x32x16_bf16 v[16:31], v[68:71], v[40:43], v[16:31]\n\t" \
  "ds_read_b128 v[92:95], %[xa3]\n\t" \
  "ds_read_b64_tr_b16 v[120:121], %[wa] offset:9216\n\t" \
  "ds_read_b64_tr_b16 v[122:123], %[wa] offset:9984\n\t" \
  "v_mfma_f32_32x32x16_bf16 v[0:15], v[72:75], v[44:47], v[0:15]\n\t" \
  "ds_read_b64_tr_b16 v[124:125], %[wa] offset:9280\n\t" \
  "ds_read_b64_tr_b16 v[126:127], %[wa] offset:10048\n\t" \
  "v_mfma_f32_32x32x16_bf16 v[16:31], v[76:79], v[44:47], v[16:31]\n\t" \
  "s_mov_b32 %[dl], 28672\n\t" \
  "s_add_u32 %[sl], %[sl], 1\n\t" \
  "s_cmp_eq_u32 %[sl], 5\n\t" \
  "s_cselect_b32 %[dl], 4294852608, %[dl]\n\t" \
  "s_cselect_b32 %[sl], 0, %[sl]\n\t" \
  "v_add_u32 %[xa0], %[dl], %[xa0]\n\t" \
  "v_add_u32 %[xa1], %[dl], %[xa1]\n\t" \
  "v_add_u32 %[xa2], %[dl], %[xa2]\n\t" \
  "v_add_u32 %[xa3], %[dl], %[xa3]\n\t" \
  "v_add_u32 %[wa], %[dl], %[wa]\n\t" \
  "s_cmp_gt_u32 %[s], 3\n\t" \
  "s_cbranch_scc1 20f\n\t" \
  "s_add_u32 m0, %[dst], %[xrel]\n\t" \
  "s_nop 0\n\t" \
  "global_load_lds_dwordx4 %[vo0], s[40:41] offset:0\n\t" \
  "global_load_lds_dwordx4 %[vo1], s[40:41] offset:1024\n\t" \
  "s_add_u32 s40, s40, 128\n\t" \
  "s_addc_u32 s41, s41, 0\n\t" \
  "s_add_u32 %[dst], %[dst], 28672\n\t" \
  "s_cmp_eq_u32 %[s], 0\n\t" \
  "s_cbranch_scc0 20f\n\t" \
  "s_sub_u32 %[dst], %[dst], 143360\n\t" \
  "20:\n\t" \
  "s_add_u32 %[s], %[s], 1\n\t" \
  "s_cmp_lt_u32 %[s], 5\n\t" \
  "s_cbranch_scc1 7f\n\t" \
  "s_cmp_eq_u32 %[s], 5\n\t" \
  "s_cbranch_scc1 9f\n\t" \
  "s_waitcnt vmcnt(0) lgkmcnt(0)\n\t" \
  "s_branch 10f\n\t" \
  "7:\n\t" \
  "s_waitcnt vmcnt(4) lgkmcnt(0)\n\t" \
  "s_branch 10f\n\t" \
  "9:\n\t" \
  "s_waitcnt vmcnt(2) lgkmcnt(0)\n\t" \
  "10:\n\t" \
  "s_barrier\n\t" \
  "s_cmp_eq_u32 %[s], 7\n\t" \
  "s_cbranch_scc1 23f\n\t" \
  "v_mfma_f32_32x32x16_bf16 v[0:15], v[96:99], v[80:83], v[0:15]\n\t" \
  "ds_read_b128 v[32:35], %[xa0]\n\t" \
  "ds_read_b64_tr_b16 v[48:49], %[wa] offset:0\n\t" \
  "ds_read_b64_tr_b16 v[50:51], %[wa] offset:768\n\t" \
  "v_mfma_f32_32x32x16_bf16 v[16:31], v[100:103], v[80:83], v[16:31]\n\t" \
  "ds_read_b64_tr_b16 v[52:53], %[wa] offset:64\n\t" \
  "ds_read_b64_tr_b16 v[54:55], %[wa] offset:832\n\t" \
  "ds_read_b128 v[36:39], %[xa1]\n\t" \
  "v_mfma_f32_32x32x16_bf16 v[0:15], v[104:107], v[84:87], v[0:15]\n\t" \
  "ds_read_b64_tr_b16 v[56:57], %[wa] offset:3072\n\t" \
  "ds_read_b64_tr_b16 v[58:59], %[wa] offset:3840\n\t" \
  "ds_read_b64_tr_b16 v[60:61], %[wa] offset:3136\n\t" \
  "v_mfma_f32_32x32x16_bf16 v[16:31], v[108:111], v[84:87], v[16:31]\n\t" \
  "ds_read_b64_tr_b16 v[62:63], %[wa] offset:3904\n\t" \
  "ds_read_b128 v[40:43], %[xa2]\n\t" \
  "ds_read_b64_tr_b16 v[64:65], %[wa] offset:6144\n\t" \
  "v_mfma_f32_32x32x16_bf16 v[0:15], v[112:115], v[88:91], v[0:15]\n\t" \
  "ds_read_b64_tr_b16 v[66:67], %[wa] offset:6912\n\t" \
  "ds_read_b64_tr_b16 v[68:69], %[wa] offset:6208\n\t" \
  "ds_read_b64_tr_b16 v[70:71], %[wa] offset:6976\n\t" \
  "v_mfma_f32_32x32x16_bf16 v[16:31], v[116:119], v[88:91], v[16:31]\n\t" \
  "ds_read_b128 v[44:47], %[xa3]\n\t" \
  "ds_read_b64_tr_b16 v[72:73], %[wa] offset:9216\n\t" \
  "ds_read_b64_tr_b16 v[74:75], %[wa] offset:9984\n\t" \
  "v_mfma_f32_32x32x16_bf16 v[0:15], v[120:123], v[92:95], v[0:15]\n\t" \
  "ds_read_b64_tr_b16 v[76:77], %[wa] offset:9280\n\t" \
  "ds_read_b64_tr_b16 v[78:79], %[wa] offset:10048\n\t" \
  "v_mfma_f32_32x32x16_bf16 v[16:31], v[124:127], v[92:95], v[16:31]\n\t" \
  "s_mov_b32 %[dl], 28672\n\t" \
  "s_add_u32 %[sl], %[sl], 1\n\t" \
  "s_cmp_eq_u32 %[sl], 5\n\t" \
  "s_cselect_b32 %[dl], 4294852608, %[dl]\n\t" \
  "s_cselect_b32 %[sl], 0, %[sl]\n\t" \
  "v_add_u32 %[xa0], %[dl], %[xa0]\n\t" \
  "v_add_u32 %[xa1], %[dl], %[xa1]\n\t" \
  "v_add_u32 %[xa2], %[dl], %[xa2]\n\t" \
  "v_add_u32 %[xa3], %[dl], %[xa3]\n\t" \
  "v_add_u32 %[wa], %[dl], %[wa]\n\t" \
  "s_cmp_gt_u32 %[s], 3\n\t" \
  "s_cbranch_scc1 21f\n\t" \
  "s_add_u32 m0, %[dst], %[xrel]\n\t" \
  "s_nop 0\n\t" \
  "global_load_lds_dwordx4 %[vo0], s[40:41] offset:0\n\t" \
  "global_load_lds_dwordx4 %[vo1], s[40:41] offset:1024\n\t" \
  "s_add_u32 s40, s40, 128\n\t" \
  "s_addc_u32 s41, s41, 0\n\t" \
  "s_add_u32 %[dst], %[dst], 28672\n\t" \
  "s_cmp_eq_u32 %[s], 0\n\t" \
  "s_cbranch_scc0 21f\n\t" \
  "s_sub_u32 %[dst], %[dst], 143360\n\t" \
  "21:\n\t" \
  "s_add_u32 %[s], %[s], 1\n\t" \
  "s_branch 2b\n\t" \
  "23:\n\t" \
  "v_mfma_f32_32x32x16_bf16 v[0:15], v[96:99], v[80:83], v[0:15]\n\t" \
  "v_mfma_f32_32x32x16_bf16 v[16:31], v[100:103], v[80:83], v[16:31]\n\t" \
  "v_mfma_f32_32x32x16_bf16 v[0:15], v[104:107], v[84:87], v[0:15]\n\t" \
  "v_mfma_f32_32x32x16_bf16 v[16:31], v[108:111], v[84:87], v[16:31]\n\t" \
  "v_mfma_f32_32x32x16_bf16 v[0:15], v[112:115], v[88:91], v[0:15]\n\t" \
  "v_mfma_f32_32x32x16_bf16 v[16:31], v[116:119], v[88:91], v[16:31]\n\t" \
  "v_mfma_f32_32x32x16_bf16 v[0:15], v[120:123], v[92:95], v[0:15]\n\t" \
  "v_mfma_f32_32x32x16_bf16 v[16:31], v[124:127], v[92:95], v[16:31]\n\t" \
  "s_mov_b32 m0, %[m0s]\n\t" \
  "s_nop 7\n\t" \
  "s_nop 3\n\t" \
  ""
#define PAN_CLOB "v32", "v33", "v34", "v35", "v36", "v37", "v38", "v39", "v40", "v41", "v42", "v43", "v44", "v45", "v46", "v47", "v48", "v49", "v50", "v51", "v52", "v53", "v54", "v55", "v56", "v57", "v58", "v59", "v60", "v61", "v62", "v63", "v64", "v65", "v66", "v67", "v68", "v69", "v70", "v71", "v72", "v73", "v74", "v75", "v76", "v77", "v78", "v79", "v80", "v81", "v82", "v83", "v84", "v85", "v86", "v87", "v88", "v89", "v90", "v91", "v92", "v93", "v94", "v95", "v96", "v97", "v98", "v99", "v100", "v101", "v102", "v103", "v104", "v105", "v106", "v107", "v108", "v109", "v110", "v111", "v112", "v113", "v114", "v115", "v116", "v117", "v118", "v119", "v120", "v121", "v122", "v123", "v124", "v125", "v126", "v127"
#define PAV_ASM \
  "s_mov_b32 %[m0s], m0\n\t" \
  "v_mov_b32 v80, 0\n\t" \
  "v_mov_b32 v81, 0\n\t" \
  "v_mov_b32 v82, 0\n\t" \
  "v_mov_b32 v83, 0\n\t" \
  "s_nop 1\n\t" \
  "v_mfma_f32_32x32x16_bf16 v[0:15], v[80:83], v[80:83], 0\n\t" \
  "v_mfma_f32_32x32x16_bf16 v[16:31], v[80:83], v[80:83], 0\n\t" \
  "s_waitcnt vmcnt(6)\n\t" \
  "s_barrier\n\t" \
  "s_mov_b32 %[s], 0\n\t" \
  "s_mov_b32 %[sl], 0\n\t" \
  "ds_read_b128 v[32:35], %[xa0]\n\t" \
  "ds_read_b64_tr_b16 v[48:49], %[wa] offset:0\n\t" \
  "ds_read_b64_tr_b16 v[50:51], %[wa] offset:768\n\t" \
  "ds_read_b64_tr_b16 v[52:53], %[wa] offset:64\n\t" \
  "ds_read_b64_tr_b16 v[54:55], %[wa] offset:832\n\t" \
  "ds_read_b128 v[36:39], %[xa1]\n\t" \
  "ds_read_b64_tr_b16 v[56:57], %[wa] offset:3072\n\t" \
  "ds_read_b64_tr_b16 v[58:59], %[wa] offset:3840\n\t" \
  "ds_read_b64_tr_b16 v[60:61], %[wa] offset:3136\n\t" \
  "ds_read_b64_tr_b16 v[62:63], %[wa] offset:3904\n\t" \
  "ds_read_b128 v[40:43], %[xa2]\n\t" \
  "ds_read_b64_tr_b16 v[64:65], %[wa] offset:6144\n\t" \
  "ds_read_b64_tr_b16 v[66:67], %[wa] offset:6912\n\t" \
  "ds_read_b64_tr_b16 v[68:69], %[wa] offset:6208\n\t" \
  "ds_read_b64_tr_b16 v[70:71], %[wa] offset:6976\n\t" \
  "ds_read_b128 v[44:47], %[xa3]\n\t" \
  "ds_read_b64_tr_b16 v[72:73], %[wa] offset:9216\n\t" \
  "ds_read_b64_tr_b16 v[74:75], %[wa] offset:9984\n\t" \
  "ds_read_b64_tr_b16 v[76:77], %[wa] offset:9280\n\t" \
  "ds_read_b64_tr_b16 v[78:79], %[wa] offset:10048\n\t" \
  "s_mov_b32 %[dl], 28672\n\t" \
  "s_add_u32 %[sl], %[sl], 1\n\t" \
  "s_cmp_eq_u32 %[sl], 5\n\t" \
  "s_cselect_b32 %[dl], 4294852608, %[dl]\n\t" \
  "s_cselect_b32 %[sl], 0, %[sl]\n\t" \
  "v_add_u32 %[xa0], %[dl], %[xa0]\n\t" \
  "v_add_u32 %[xa1], %[dl], %[xa1]\n\t" \
  "v_add_u32 %[xa2], %[dl], %[xa2]\n\t" \
  "v_add_u32 %[xa3], %[dl], %[xa3]\n\t" \
  "v_add_u32 %[wa], %[dl], %[wa]\n\t" \
  "2:\n\t" \
  "s_cmp_lt_u32 %[s], 5\n\t" \
  "s_cbranch_scc1 3f\n\t" \
  "s_cmp_eq_u32 %[s], 5\n\t" \
  "s_cbranch_scc1 5f\n\t" \
  "s_waitcnt vmcnt(0) lgkmcnt(0)\n\t" \
  "s_branch 6f\n\t" \
  "3:\n\t" \
  "s_waitcnt vmcnt(4) lgkmcnt(0)\n\t" \
  "s_branch 6f\n\t" \
  "5:\n\t" \
  "s_waitcnt vmcnt(2) lgkmcnt(0)\n\t" \
  "6:\n\t" \
  "s_barrier\n\t" \
  "v_mfma_f32_32x32x16_bf16 v[0:15], v[32:35], v[48:51], v[0:15]\n\t" \
  "ds_read_b128 v[80:83], %[xa0]\n\t" \
  "ds_read_b64_tr_b16 v[96:97], %[wa] offset:0\n\t" \
  "ds_read_b64_tr_b16 v[98:99], %[wa] offset:768\n\t" \
  "v_mfma_f32_32x32x16_bf16 v[16:31], v[32:35], v[52:55], v[16:31]\n\t" \
  "ds_read_b64_tr_b16 v[100:101], %[wa] offset:64\n\t" \
  "ds_read_b64_tr_b16 v[102:103], %[wa] offset:832\n\t" \
  "ds_read_b128 v[84:87], %[xa1]\n\t" \
  "v_mfma_f32_32x32x16_bf16 v[0:15], v[36:39], v[56:59], v[0:15]\n\t" \
  "ds_read_b64_tr_b16 v[104:105], %[wa] offset:3072\n\t" \
  "ds_read_b64_tr_b16 v[106:107], %[wa] offset:3840\n\t" \
  "ds_read_b64_tr_b16 v[108:109], %[wa] offset:3136\n\t" \
  "v_mfma_f32_32x32x16_bf16 v[16:31], v[36:39], v[60:63], v[16:31]\n\t" \
  "ds_read_b64_tr_b16 v[110:111], %[wa] offset:3904\n\t" \
  "ds_read_b128 v[88:91], %[xa2]\n\t" \
  "ds_read_b64_tr_b16 v[112:113], %[wa] offset:6144\n\t" \
  "v_mfma_f32_32x32x16_bf16 v[0:15], v[40:43], v[64:67], v[0:15]\n\t" \
  "ds_read_b64_tr_b16 v[114:115], %[wa] offset:6912\n\t" \
  "ds_read_b64_tr_b16 v[116:117], %[wa] offset:6208\n\t" \
  "ds_read_b64_tr_b16 v[118:119], %[wa] offset:6976\n\t" \
  "v_mfma_f32_32x32x16_bf16 v[16:31], v[40:43], v[68:71], v[16:31]\n\t" \
  "ds_read_b128 v[92:95], %[xa3]\n\t" \
  "ds_read_b64_tr_b16 v[120:121], %[wa] offset:9216\n\t" \
  "ds_read_b64_tr_b16 v[122:123], %[wa] offset:9984\n\t" \
  "v_mfma_f32_32x32x16_bf16 v[0:15], v[44:47], v[72:75], v[0:15]\n\t" \
  "ds_read_b64_tr_b16 v[124:125], %[wa] offset:9280\n\t" \
  "ds_read_b64_tr_b16 v[126:127], %[wa] offset:10048\n\t" \
  "v_mfma_f32_32x32x16_bf16 v[16:31], v[44:47], v[76:79], v[16:31]\n\t" \
  "s_mov_b32 %[dl], 28672\n\t" \
  "s_add_u32 %[sl], %[sl], 1\n\t" \
  "s_cmp_eq_u32 %[sl], 5\n\t" \
  "s_cselect_b32 %[dl], 4294852608, %[dl]\n\t" \
  "s_cselect_b32 %[sl], 0, %[sl]\n\t" \
  "v_add_u32 %[xa0], %[dl], %[xa0]\n\t" \
  "v_add_u32 %[xa1], %[dl], %[xa1]\n\t" \
  "v_add_u32 %[xa2], %[dl], %[xa2]\n\t" \
  "v_add_u32 %[xa3], %[dl], %[xa3]\n\t" \
  "v_add_u32 %[wa], %[dl], %[wa]\n\t" \
  "s_cmp_gt_u32 %[s], 3\n\t" \
  "s_cbranch_scc1 20f\n\t" \
  "s_add_u32 m0, %[dst], %[xrel]\n\t" \
  "s_nop 0\n\t" \
  "global_load_lds_dwordx4 %[vo0], s[40:41] offset:0\n\t" \
  "global_load_lds_dwordx4 %[vo1], s[40:41] offset:1024\n\t" \
  "s_add_u32 s40, s40, 128\n\t" \
  "s_addc_u32 s41, s41, 0\n\t" \
  "s_add_u32 %[dst], %[dst], 28672\n\t" \
  "s_cmp_eq_u32 %[s], 0\n\t" \
  "s_cbranch_scc0 20f\n\t" \
  "s_sub_u32 %[dst], %[dst], 143360\n\t" \
  "20:\n\t" \
  "s_add_u32 %[s], %[s], 1\n\t" \
  "s_cmp_lt_u32 %[s], 5\n\t" \
  "s_cbranch_scc1 7f\n\t" \
  "s_cmp_eq_u32 %[s], 5\n\t" \
  "s_cbranch_scc1 9f\n\t" \
  "s_waitcnt vmcnt(0) lgkmcnt(0)\n\t" \
  "s_branch 10f\n\t" \
  "7:\n\t" \
  "s_waitcnt vmcnt(4) lgkmcnt(0)\n\t" \
  "s_branch 10f\n\t" \
  "9:\n\t" \
  "s_waitcnt vmcnt(2) lgkmcnt(0)\n\t" \
  "10:\n\t" \
  "s_barrier\n\t" \
  "s_cmp_eq_u32 %[s], 7\n\t" \
  "s_cbranch_scc1 23f\n\t" \
  "v_mfma_f32_32x32x16_bf16 v[0:15], v[80:83], v[96:99], v[0:15]\n\t" \
  "ds_read_b128 v[32:35], %[xa0]\n\t" \
  "ds_read_b64_tr_b16 v[48:49], %[wa] offset:0\n\t" \
  "ds_read_b64_tr_b16 v[50:51], %[wa] offset:768\n\t" \
  "v_mfma_f32_32x32x16_bf16 v[16:31], v[80:83], v[100:103], v[16:31]\n\t" \
  "ds_read_b64_tr_b16 v[52:53], %[wa] offset:64\n\t" \
  "ds_read_b64_tr_b16 v[54:55], %[wa] offset:832\n\t" \
  "ds_read_b128 v[36:39], %[xa1]\n\t" \
  "v_mfma_f32_32x32x16_bf16 v[0:15], v[84:87], v[104:107], v[0:15]\n\t" \
  "ds_read_b64_tr_b16 v[56:57], %[wa] offset:3072\n\t" \
  "ds_read_b64_tr_b16 v[58:59], %[wa] offset:3840\n\t" \
  "ds_read_b64_tr_b16 v[60:61], %[wa] offset:3136\n\t" \
  "v_mfma_f32_32x32x16_bf16 v[16:31], v[84:87], v[108:111], v[16:31]\n\t" \
  "ds_read_b64_tr_b16 v[62:63], %[wa] offset:3904\n\t" \
  "ds_read_b128 v[40:43], %[xa2]\n\t" \
  "ds_read_b64_tr_b16 v[64:65], %[wa] offset:6144\n\t" \
  "v_mfma_f32_32x32x16_bf16 v[0:15], v[88:91], v[112:115], v[0:15]\n\t" \
  "ds_read_b64_tr_b16 v[66:67], %[wa] offset:6912\n\t" \
  "ds_read_b64_tr_b16 v[68:69], %[wa] offset:6208\n\t" \
  "ds_read_b64_tr_b16 v[70:71], %[wa] offset:6976\n\t" \
  "v_mfma_f32_32x32x16_bf16 v[16:31], v[88:91], v[116:119], v[16:31]\n\t" \
  "ds_read_b128 v[44:47], %[xa3]\n\t" \
  "ds_read_b64_tr_b16 v[72:73], %[wa] offset:9216\n\t" \
  "ds_read_b64_tr_b16 v[74:75], %[wa] offset:9984\n\t" \
  "v_mfma_f32_32x32x16_bf16 v[0:15], v[92:95], v[120:123], v[0:15]\n\t" \
  "ds_read_b64_tr_b16 v[76:77], %[wa] offset:9280\n\t" \
  "ds_read_b64_tr_b16 v[78:79], %[wa] offset:10048\n\t" \
  "v_mfma_f32_32x32x16_bf16 v[16:31], v[92:95], v[124:127], v[16:31]\n\t" \
  "s_mov_b32 %[dl], 28672\n\t" \
  "s_add_u32 %[sl], %[sl], 1\n\t" \
  "s_cmp_eq_u32 %[sl], 5\n\t" \
  "s_cselect_b32 %[dl], 4294852608, %[dl]\n\t" \
  "s_cselect_b32 %[sl], 0, %[sl]\n\t" \
  "v_add_u32 %[xa0], %[dl], %[xa0]\n\t" \
  "v_add_u32 %[xa1], %[dl], %[xa1]\n\t" \
  "v_add_u32 %[xa2], %[dl], %[xa2]\n\t" \
  "v_add_u32 %[xa3], %[dl], %[xa3]\n\t" \
  "v_add_u32 %[wa], %[dl], %[wa]\n\t" \
  "s_cmp_gt_u32 %[s], 3\n\t" \
  "s_cbranch_scc1 21f\n\t" \
  "s_add_u32 m0, %[dst], %[xrel]\n\t" \
  "s_nop 0\n\t" \
  "global_load_lds_dwordx4 %[vo0], s[40:41] offset:0\n\t" \
  "global_load_lds_dwordx4 %[vo1], s[40:41] offset:1024\n\t" \
  "s_add_u32 s40, s40, 128\n\t" \
  "s_addc_u32 s41, s41, 0\n\t" \
  "s_add_u32 %[dst], %[dst], 28672\n\t" \
  "s_cmp_eq_u32 %[s], 0\n\t" \
  "s_cbranch_scc0 21f\n\t" \
  "s_sub_u32 %[dst], %[dst], 143360\n\t" \
  "21:\n\t" \
  "s_add_u32 %[s], %[s], 1\n\t" \
  "s_branch 2b\n\t" \
  "23:\n\t" \
  "v_mfma_f32_32x32x16_bf16 v[0:15], v[80:83], v[96:99], v[0:15]\n\t" \
  "v_mfma_f32_32x32x16_bf16 v[16:31], v[80:83], v[100:103], v[16:31]\n\t" \
  "v_mfma_f32_32x32x16_bf16 v[0:15], v[84:87], v[104:107], v[0:15]\n\t" \
  "v_mfma_f32_32x32x16_bf16 v[16:31], v[84:87], v[108:111], v[16:31]\n\t" \
  "v_mfma_f32_32x32x16_bf16 v[0:15], v[88:91], v[112:115], v[0:15]\n\t" \
  "v_mfma_f32_32x32x16_bf16 v[16:31], v[88:91], v[116:119], v[16:31]\n\t" \
  "v_mfma_f32_32x32x16_bf16 v[0:15], v[92:95], v[120:123], v[0:15]\n\t" \
  "v_mfma_f32_32x32x16_bf16 v[16:31], v[92:95], v[124:127], v[16:31]\n\t" \
  "s_mov_b32 m0, %[m0s]\n\t" \
  "s_nop 7\n\t" \
  "s_nop 3\n\t" \
  ""
#define PAV_CLOB "v32", "v33", "v34", "v35", "v36", "v37", "v38", "v39", "v40", "v41", "v42", "v43", "v44", "v45", "v46", "v47", "v48", "v49", "v50", "v51", "v52", "v53", "v54", "v55", "v56", "v57", "v58", "v59", "v60", "v61", "v62", "v63", "v64", "v65", "v66", "v67", "v68", "v69", "v70", "v71", "v72", "v73", "v74", "v75", "v76", "v77", "v78", "v79", "v80", "v81", "v82", "v83", "v84", "v85", "v86", "v87", "v88", "v89", "v90", "v91", "v92", "v93", "v94", "v95", "v96", "v97", "v98", "v99", "v100", "v101", "v102", "v103", "v104", "v105", "v106", "v107", "v108", "v109", "v110", "v111", "v112", "v113", "v114", "v115", "v116", "v117", "v118", "v119", "v120", "v121", "v122", "v123", "v124", "v125", "v126", "v127"
#define PBN_ASM \
  "s_mov_b32 %[m0s], m0\n\t" \
  "v_mov_b32 v48, 0\n\t" \
  "v_mov_b32 v49, 0\n\t" \
  "v_mov_b32 v50, 0\n\t" \
  "v_mov_b32 v51, 0\n\t" \
  "s_nop 1\n\t" \
  "v_mfma_f32_32x32x16_bf16 v[0:15], v[48:51], v[48:51], 0\n\t" \
  "s_waitcnt vmcnt(15)\n\t" \
  "s_barrier\n\t" \
  "s_mov_b32 %[s], 0\n\t" \
  "s_mov_b32 %[sl], 0\n\t" \
  "ds_read_b128 v[16:19], %[xa0]\n\t" \
  "ds_read_b64_tr_b16 v[32:33], %[wa] offset:0\n\t" \
  "ds_read_b64_tr_b16 v[34:35], %[wa] offset:768\n\t" \
  "ds_read_b128 v[20:23], %[xa1]\n\t" \
  "ds_read_b64_tr_b16 v[36:37], %[wa] offset:3072\n\t" \
  "ds_read_b64_tr_b16 v[38:39], %[wa] offset:3840\n\t" \
  "ds_read_b128 v[24:27], %[xa2]\n\t" \
  "ds_read_b64_tr_b16 v[40:41], %[wa] offset:6144\n\t" \
  "ds_read_b64_tr_b16 v[42:43], %[wa] offset:6912\n\t" \
  "ds_read_b128 v[28:31], %[xa3]\n\t" \
  "ds_read_b64_tr_b16 v[44:45], %[wa] offset:9216\n\t" \
  "ds_read_b64_tr_b16 v[46:47], %[wa] offset:9984\n\t" \
  "s_mov_b32 %[dl], 28672\n\t" \
  "s_add_u32 %[sl], %[sl], 1\n\t" \
  "s_cmp_eq_u32 %[sl], 5\n\t" \
  "s_cselect_b32 %[dl], 4294852608, %[dl]\n\t" \
  "s_cselect_b32 %[sl], 0, %[sl]\n\t" \
  "v_add_u32 %[xa0], %[dl], %[xa0]\n\t" \
  "v_add_u32 %[xa1], %[dl], %[xa1]\n\t" \
  "v_add_u32 %[xa2], %[dl], %[xa2]\n\t" \
  "v_add_u32 %[xa3], %[dl], %[xa3]\n\t" \
  "v_add_u32 %[wa], %[dl], %[wa]\n\t" \
  "2:\n\t" \
  "s_cmp_lt_u32 %[s], 5\n\t" \
  "s_cbranch_scc1 3f\n\t" \
  "s_cmp_eq_u32 %[s], 5\n\t" \
  "s_cbranch_scc1 5f\n\t" \
  "s_waitcnt vmcnt(0) lgkmcnt(0)\n\t" \
  "s_branch 6f\n\t" \
  "3:\n\t" \
  "s_waitcnt vmcnt(10) lgkmcnt(0)\n\t" \
  "s_branch 6f\n\t" \
  "5:\n\t" \
  "s_waitcnt vmcnt(5) lgkmcnt(0)\n\t" \
  "6:\n\t" \
  "s_barrier\n\t" \
  "v_mfma_f32_32x32x16_bf16 v[0:15], v[32:35], v[16:19], v[0:15]\n\t" \
  "ds_read_b128 v[48:51], %[xa0]\n\t" \
  "ds_read_b64_tr_b16 v[64:65], %[wa] offset:0\n\t" \
  "ds_read_b64_tr_b16 v[66:67], %[wa] offset:768\n\t" \
  "v_mfma_f32_32x32x16_bf16 v[0:15], v[36:39], v[20:23], v[0:15]\n\t" \
  "ds_read_b128 v[52:55], %[xa1]\n\t" \
  "ds_read_b64_tr_b16 v[68:69], %[wa] offset:3072\n\t" \
  "ds_read_b64_tr_b16 v[70:71], %[wa] offset:3840\n\t" \
  "v_mfma_f32_32x32x16_bf16 v[0:15], v[40:43], v[24:27], v[0:15]\n\t" \
  "ds_read_b128 v[56:59], %[xa2]\n\t" \
  "ds_read_b64_tr_b16 v[72:73], %[wa] offset:6144\n\t" \
  "ds_read_b64_tr_b16 v[74:75], %[wa] offset:6912\n\t" \
  "v_mfma_f32_32x32x16_bf16 v[0:15], v[44:47], v[28:31], v[0:15]\n\t" \
  "ds_read_b128 v[60:63], %[xa3]\n\t" \
  "ds_read_b64_tr_b16 v[76:77], %[wa] offset:9216\n\t" \
  "ds_read_b64_tr_b16 v[78:79], %[wa] offset:9984\n\t" \
  "s_mov_b32 %[dl], 28672\n\t" \
  "s_add_u32 %[sl], %[sl], 1\n\t" \
  "s_cmp_eq_u32 %[sl], 5\n\t" \
  "s_cselect_b32 %[dl], 4294852608, %[dl]\n\t" \
  "s_cselect_b32 %[sl], 0, %[sl]\n\t" \
  "v_add_u32 %[xa0], %[dl], %[xa0]\n\t" \
  "v_add_u32 %[xa1], %[dl], %[xa1]\n\t" \
  "v_add_u32 %[xa2], %[dl], %[xa2]\n\t" \
  "v_add_u32 %[xa3], %[dl], %[xa3]\n\t" \
  "v_add_u32 %[wa], %[dl], %[wa]\n\t" \
  "s_cmp_gt_u32 %[s], 3\n\t" \
  "s_cbranch_scc1 20f\n\t" \
  "s_add_u32 m0, %[dst], %[xrel]\n\t" \
  "s_nop 0\n\t" \
  "global_load_lds_dwordx4 %[vo0], s[40:41] offset:0\n\t" \
  "global_load_lds_dwordx4 %[vo1], s[40:41] offset:1024\n\t" \
  "s_add_u32 m0, %[dst], %[wrel]\n\t" \
  "s_nop 0\n\t" \
  "global_load_lds_dwordx4 %[vo2], s[42:43] offset:0\n\t" \
  "global_load_lds_dwordx4 %[vo3], s[42:43] offset:1024\n\t" \
  "global_load_lds_dwordx4 %[vo4], s[42:43] offset:2048\n\t" \
  "s_add_u32 s42, s42, 196608\n\t" \
  "s_addc_u32 s43, s43, 0\n\t" \
  "s_add_u32 s40, s40, 128\n\t" \
  "s_addc_u32 s41, s41, 0\n\t" \
  "s_add_u32 %[dst], %[dst], 28672\n\t" \
  "s_cmp_eq_u32 %[s], 0\n\t" \
  "s_cbranch_scc0 20f\n\t" \
  "s_sub_u32 %[dst], %[dst], 143360\n\t" \
  "20:\n\t" \
  "s_add_u32 %[s], %[s], 1\n\t" \
  "s_cmp_lt_u32 %[s], 5\n\t" \
  "s_cbranch_scc1 7f\n\t" \
  "s_cmp_eq_u32 %[s], 5\n\t" \
  "s_cbranch_scc1 9f\n\t" \
  "s_waitcnt vmcnt(0) lgkmcnt(0)\n\t" \
  "s_branch 10f\n\t" \
  "7:\n\t" \
  "s_waitcnt vmcnt(10) lgkmcnt(0)\n\t" \
  "s_branch 10f\n\t" \
  "9:\n\t" \
  "s_waitcnt vmcnt(5) lgkmcnt(0)\n\t" \
  "10:\n\t" \
  "s_barrier\n\t" \
  "s_cmp_eq_u32 %[s], 7\n\t" \
  "s_cbranch_scc1 23f\n\t" \
  "v_mfma_f32_32x32x16_bf16 v[0:15], v[64:67], v[48:51], v[0:15]\n\t" \
  "ds_read_b128 v[16:19], %[xa0]\n\t" \
  "ds_read_b64_tr_b16 v[32:33], %[wa] offset:0\n\t" \
  "ds_read_b64_tr_b16 v[34:35], %[wa] offset:768\n\t" \
  "v_mfma_f32_32x32x16_bf16 v[0:15], v[68:71], v[52:55], v[0:15]\n\t" \
  "ds_read_b128 v[20:23], %[xa1]\n\t" \
  "ds_read_b64_tr_b16 v[36:37], %[wa] offset:3072\n\t" \
  "ds_read_b64_tr_b16 v[38:39], %[wa] offset:3840\n\t" \
  "v_mfma_f32_32x32x16_bf16 v[0:15], v[72:75], v[56:59], v[0:15]\n\t" \
  "ds_read_b128 v[24:27], %[xa2]\n\t" \
  "ds_read_b64_tr_b16 v[40:41], %[wa] offset:6144\n\t" \
  "ds_read_b64_tr_b16 v[42:43], %[wa] offset:6912\n\t" \
  "v_mfma_f32_32x32x16_bf16 v[0:15], v[76:79], v[60:63], v[0:15]\n\t" \
  "ds_read_b128 v[28:31], %[xa3]\n\t" \
  "ds_read_b64_tr_b16 v[44:45], %[wa] offset:9216\n\t" \
  "ds_read_b64_tr_b16 v[46:47], %[wa] offset:9984\n\t" \
  "s_mov_b32 %[dl], 28672\n\t" \
  "s_add_u32 %[sl], %[sl], 1\n\t" \
  "s_cmp_eq_u32 %[sl], 5\n\t" \
  "s_cselect_b32 %[dl], 4294852608, %[dl]\n\t" \
  "s_cselect_b32 %[sl], 0, %[sl]\n\t" \
  "v_add_u32 %[xa0], %[dl], %[xa0]\n\t" \
  "v_add_u32 %[xa1], %[dl], %[xa1]\n\t" \
  "v_add_u32 %[xa2], %[dl], %[xa2]\n\t" \
  "v_add_u32 %[xa3], %[dl], %[xa3]\n\t" \
  "v_add_u32 %[wa], %[dl], %[wa]\n\t" \
  "s_cmp_gt_u32 %[s], 3\n\t" \
  "s_cbranch_scc1 21f\n\t" \
  "s_add_u32 m0, %[dst], %[xrel]\n\t" \
  "s_nop 0\n\t" \
  "global_load_lds_dwordx4 %[vo0], s[40:41] offset:0\n\t" \
  "global_load_lds_dwordx4 %[vo1], s[40:41] offset:1024\n\t" \
  "s_add_u32 m0, %[dst], %[wrel]\n\t" \
  "s_nop 0\n\t" \
  "global_load_lds_dwordx4 %[vo2], s[42:43] offset:0\n\t" \
  "global_load_lds_dwordx4 %[vo3], s[42:43] offset:1024\n\t" \
  "global_load_lds_dwordx4 %[vo4], s[42:43] offset:2048\n\t" \
  "s_add_u32 s42, s42, 196608\n\t" \
  "s_addc_u32 s43, s43, 0\n\t" \
  "s_add_u32 s40, s40, 128\n\t" \
  "s_addc_u32 s41, s41, 0\n\t" \
  "s_add_u32 %[dst], %[dst], 28672\n\t" \
  "s_cmp_eq_u32 %[s], 0\n\t" \
  "s_cbranch_scc0 21f\n\t" \
  "s_sub_u32 %[dst], %[dst], 143360\n\t" \
  "21:\n\t" \
  "s_add_u32 %[s], %[s], 1\n\t" \
  "s_branch 2b\n\t" \
  "23:\n\t" \
  "v_mfma_f32_32x32x16_bf16 v[0:15], v[64:67], v[48:51], v[0:15]\n\t" \
  "v_mfma_f32_32x32x16_bf16 v[0:15], v[68:71], v[52:55], v[0:15]\n\t" \
  "v_mfma_f32_32x32x16_bf16 v[0:15], v[72:75], v[56:59], v[0:15]\n\t" \
  "v_mfma_f32_32x32x16_bf16 v[0:15], v[76:79], v[60:63], v[0:15]\n\t" \
  "s_mov_b32 m0, %[m0s]\n\t" \
  "s_nop 7\n\t" \
  "s_nop 3\n\t" \
  ""
#define PBN_CLOB "v16", "v17", "v18", "v19", "v20", "v21", "v22", "v23", "v24", "v25", "v26", "v27", "v28", "v29", "v30", "v31", "v32", "v33", "v34", "v35", "v36", "v37", "v38", "v39", "v40", "v41", "v42", "v43", "v44", "v45", "v46", "v47", "v48", "v49", "v50", "v51", "v52", "v53", "v54", "v55", "v56", "v57", "v58", "v59", "v60", "v61", "v62", "v63", "v64", "v65", "v66", "v67", "v68", "v69", "v70", "v71", "v72", "v73", "v74", "v75", "v76", "v77", "v78", "v79"
#define PBV_ASM \
  "s_mov_b32 %[m0s], m0\n\t" \
  "v_mov_b32 v48, 0\n\t" \
  "v_mov_b32 v49, 0\n\t" \
  "v_mov_b32 v50, 0\n\t" \
  "v_mov_b32 v51, 0\n\t" \
  "s_nop 1\n\t" \
  "v_mfma_f32_32x32x16_bf16 v[0:15], v[48:51], v[48:51], 0\n\t" \
  "s_waitcnt vmcnt(15)\n\t" \
  "s_barrier\n\t" \
  "s_mov_b32 %[s], 0\n\t" \
  "s_mov_b32 %[sl], 0\n\t" \
  "ds_read_b128 v[16:19], %[xa0]\n\t" \
  "ds_read_b64_tr_b16 v[32:33], %[wa] offset:0\n\t" \
  "ds_read_b64_tr_b16 v[34:35], %[wa] offset:768\n\t" \
  "ds_read_b128 v[20:23], %[xa1]\n\t" \
  "ds_read_b64_tr_b16 v[36:37], %[wa] offset:3072\n\t" \
  "ds_read_b64_tr_b16 v[38:39], %[wa] offset:3840\n\t" \
  "ds_read_b128 v[24:27], %[xa2]\n\t" \
  "ds_read_b64_tr_b16 v[40:41], %[wa] offset:6144\n\t" \
  "ds_read_b64_tr_b16 v[42:43], %[wa] offset:6912\n\t" \
  "ds_read_b128 v[28:31], %[xa3]\n\t" \
  "ds_read_b64_tr_b16 v[44:45], %[wa] offset:9216\n\t" \
  "ds_read_b64_tr_b16 v[46:47], %[wa] offset:9984\n\t" \
  "s_mov_b32 %[dl], 28672\n\t" \
  "s_add_u32 %[sl], %[sl], 1\n\t" \
  "s_cmp_eq_u32 %[sl], 5\n\t" \
  "s_cselect_b32 %[dl], 4294852608, %[dl]\n\t" \
  "s_cselect_b32 %[sl], 0, %[sl]\n\t" \
  "v_add_u32 %[xa0], %[dl], %[xa0]\n\t" \
  "v_add_u32 %[xa1], %[dl], %[xa1]\n\t" \
  "v_add_u32 %[xa2], %[dl], %[xa2]\n\t" \
  "v_add_u32 %[xa3], %[dl], %[xa3]\n\t" \
  "v_add_u32 %[wa], %[dl], %[wa]\n\t" \
  "2:\n\t" \
  "s_cmp_lt_u32 %[s], 5\n\t" \
  "s_cbranch_scc1 3f\n\t" \
  "s_cmp_eq_u32 %[s], 5\n\t" \
  "s_cbranch_scc1 5f\n\t" \
  "s_waitcnt vmcnt(0) lgkmcnt(0)\n\t" \
  "s_branch 6f\n\t" \
  "3:\n\t" \
  "s_waitcnt vmcnt(10) lgkmcnt(0)\n\t" \
  "s_branch 6f\n\t" \
  "5:\n\t" \
  "s_waitcnt vmcnt(5) lgkmcnt(0)\n\t" \
  "6:\n\t" \
  "s_barrier\n\t" \
  "v_mfma_f32_32x32x16_bf16 v[0:15], v[16:19], v[32:35], v[0:15]\n\t" \
  "ds_read_b128 v[48:51], %[xa0]\n\t" \
  "ds_read_b64_tr_b16 v[64:65], %[wa] offset:0\n\t" \
  "ds_read_b64_tr_b16 v[66:67], %[wa] offset:768\n\t" \
  "v_mfma_f32_32x32x16_bf16 v[0:15], v[20:23], v[36:39], v[0:15]\n\t" \
  "ds_read_b128 v[52:55], %[xa1]\n\t" \
  "ds_read_b64_tr_b16 v[68:69], %[wa] offset:3072\n\t" \
  "ds_read_b64_tr_b16 v[70:71], %[wa] offset:3840\n\t" \
  "v_mfma_f32_32x32x16_bf16 v[0:15], v[24:27], v[40:43], v[0:15]\n\t" \
  "ds_read_b128 v[56:59], %[xa2]\n\t" \
  "ds_read_b64_tr_b16 v[72:73], %[wa] offset:6144\n\t" \
  "ds_read_b64_tr_b16 v[74:75], %[wa] offset:6912\n\t" \
  "v_mfma_f32_32x32x16_bf16 v[0:15], v[28:31], v[44:47], v[0:15]\n\t" \
  "ds_read_b128 v[60:63], %[xa3]\n\t" \
  "ds_read_b64_tr_b16 v[76:77], %[wa] offset:9216\n\t" \
  "ds_read_b64_tr_b16 v[78:79], %[wa] offset:9984\n\t" \
  "s_mov_b32 %[dl], 28672\n\t" \
  "s_add_u32 %[sl], %[sl], 1\n\t" \
  "s_cmp_eq_u32 %[sl], 5\n\t" \
  "s_cselect_b32 %[dl], 4294852608, %[dl]\n\t" \
  "s_cselect_b32 %[sl], 0, %[sl]\n\t" \
  "v_add_u32 %[xa0], %[dl], %[xa0]\n\t" \
  "v_add_u32 %[xa1], %[dl], %[xa1]\n\t" \
  "v_add_u32 %[xa2], %[dl], %[xa2]\n\t" \
  "v_add_u32 %[xa3], %[dl], %[xa3]\n\t" \
  "v_add_u32 %[wa], %[dl], %[wa]\n\t" \
  "s_cmp_gt_u32 %[s], 3\n\t" \
  "s_cbranch_scc1 20f\n\t" \
  "s_add_u32 m0, %[dst], %[xrel]\n\t" \
  "s_nop 0\n\t" \
  "global_load_lds_dwordx4 %[vo0], s[40:41] offset:0\n\t" \
  "global_load_lds_dwordx4 %[vo1], s[40:41] offset:1024\n\t" \
  "s_add_u32 m0, %[dst], %[wrel]\n\t" \
  "s_nop 0\n\t" \
  "global_load_lds_dwordx4 %[vo2], s[42:43] offset:0\n\t" \
  "global_load_lds_dwordx4 %[vo3], s[42:43] offset:1024\n\t" \
  "global_load_lds_dwordx4 %[vo4], s[42:43] offset:2048\n\t" \
  "s_add_u32 s42, s42, 196608\n\t" \
  "s_addc_u32 s43, s43, 0\n\t" \
  "s_add_u32 s40, s40, 128\n\t" \
  "s_addc_u32 s41, s41, 0\n\t" \
  "s_add_u32 %[dst], %[dst], 28672\n\t" \
  "s_cmp_eq_u32 %[s], 0\n\t" \
  "s_cbranch_scc0 20f\n\t" \
  "s_sub_u32 %[dst], %[dst], 143360\n\t" \
  "20:\n\t" \
  "s_add_u32 %[s], %[s], 1\n\t" \
  "s_cmp_lt_u32 %[s], 5\n\t" \
  "s_cbranch_scc1 7f\n\t" \
  "s_cmp_eq_u32 %[s], 5\n\t" \
  "s_cbranch_scc1 9f\n\t" \
  "s_waitcnt vmcnt(0) lgkmcnt(0)\n\t" \
  "s_branch 10f\n\t" \
  "7:\n\t" \
  "s_waitcnt vmcnt(10) lgkmcnt(0)\n\t" \
  "s_branch 10f\n\t" \
  "9:\n\t" \
  "s_waitcnt vmcnt(5) lgkmcnt(0)\n\t" \
  "10:\n\t" \
  "s_barrier\n\t" \
  "s_cmp_eq_u32 %[s], 7\n\t" \
  "s_cbranch_scc1 23f\n\t" \
  "v_mfma_f32_32x32x16_bf16 v[0:15], v[48:51], v[64:67], v[0:15]\n\t" \
  "ds_read_b128 v[16:19], %[xa0]\n\t" \
  "ds_read_b64_tr_b16 v[32:33], %[wa] offset:0\n\t" \
  "ds_read_b64_tr_b16 v[34:35], %[wa] offset:768\n\t" \
  "v_mfma_f32_32x32x16_bf16 v[0:15], v[52:55], v[68:71], v[0:15]\n\t" \
  "ds_read_b128 v[20:23], %[xa1]\n\t" \
  "ds_read_b64_tr_b16 v[36:37], %[wa] offset:3072\n\t" \
  "ds_read_b64_tr_b16 v[38:39], %[wa] offset:3840\n\t" \
  "v_mfma_f32_32x32x16_bf16 v[0:15], v[56:59], v[72:75], v[0:15]\n\t" \
  "ds_read_b128 v[24:27], %[xa2]\n\t" \
  "ds_read_b64_tr_b16 v[40:41], %[wa] offset:6144\n\t" \
  "ds_read_b64_tr_b16 v[42:43], %[wa] offset:6912\n\t" \
  "v_mfma_f32_32x32x16_bf16 v[0:15], v[60:63], v[76:79], v[0:15]\n\t" \
  "ds_read_b128 v[28:31], %[xa3]\n\t" \
  "ds_read_b64_tr_b16 v[44:45], %[wa] offset:9216\n\t" \
  "ds_read_b64_tr_b16 v[46:47], %[wa] offset:9984\n\t" \
  "s_mov_b32 %[dl], 28672\n\t" \
  "s_add_u32 %[sl], %[sl], 1\n\t" \
  "s_cmp_eq_u32 %[sl], 5\n\t" \
  "s_cselect_b32 %[dl], 4294852608, %[dl]\n\t" \
  "s_cselect_b32 %[sl], 0, %[sl]\n\t" \
  "v_add_u32 %[xa0], %[dl], %[xa0]\n\t" \
  "v_add_u32 %[xa1], %[dl], %[xa1]\n\t" \
  "v_add_u32 %[xa2], %[dl], %[xa2]\n\t" \
  "v_add_u32 %[xa3], %[dl], %[xa3]\n\t" \
  "v_add_u32 %[wa], %[dl], %[wa]\n\t" \
  "s_cmp_gt_u32 %[s], 3\n\t" \
  "s_cbranch_scc1 21f\n\t" \
  "s_add_u32 m0, %[dst], %[xrel]\n\t" \
  "s_nop 0\n\t" \
  "global_load_lds_dwordx4 %[vo0], s[40:41] offset:0\n\t" \
  "global_load_lds_dwordx4 %[vo1], s[40:41] offset:1024\n\t" \
  "s_add_u32 m0, %[dst], %[wrel]\n\t" \
  "s_nop 0\n\t" \
  "global_load_lds_dwordx4 %[vo2], s[42:43] offset:0\n\t" \
  "global_load_lds_dwordx4 %[vo3], s[42:43] offset:1024\n\t" \
  "global_load_lds_dwordx4 %[vo4], s[42:43] offset:2048\n\t" \
  "s_add_u32 s42, s42, 196608\n\t" \
  "s_addc_u32 s43, s43, 0\n\t" \
  "s_add_u32 s40, s40, 128\n\t" \
  "s_addc_u32 s41, s41, 0\n\t" \
  "s_add_u32 %[dst], %[dst], 28672\n\t" \
  "s_cmp_eq_u32 %[s], 0\n\t" \
  "s_cbranch_scc0 21f\n\t" \
  "s_sub_u32 %[dst], %[dst], 143360\n\t" \
  "21:\n\t" \
  "s_add_u32 %[s], %[s], 1\n\t" \
  "s_branch 2b\n\t" \
  "23:\n\t" \
  "v_mfma_f32_32x32x16_bf16 v[0:15], v[48:51], v[64:67], v[0:15]\n\t" \
  "v_mfma_f32_32x32x16_bf16 v[0:15], v[52:55], v[68:71], v[0:15]\n\t" \
  "v_mfma_f32_32x32x16_bf16 v[0:15], v[56:59], v[72:75], v[0:15]\n\t" \
  "v_mfma_f32_32x32x16_bf16 v[0:15], v[60:63], v[76:79], v[0:15]\n\t" \
  "s_mov_b32 m0, %[m0s]\n\t" \
  "s_nop 7\n\t" \
  "s_nop 3\n\t" \
  ""
#define PBV_CLOB "v16", "v17", "v18", "v19", "v20", "v21", "v22", "v23", "v24", "v25", "v26", "v27", "v28", "v29", "v30", "v31", "v32", "v33", "v34", "v35", "v36", "v37", "v38", "v39", "v40", "v41", "v42", "v43", "v44", "v45", "v46", "v47", "v48", "v49", "v50", "v51", "v52", "v53", "v54", "v55", "v56", "v57", "v58", "v59", "v60", "v61", "v62", "v63", "v64", "v65", "v66", "v67", "v68", "v69", "v70", "v71", "v72", "v73", "v74", "v75", "v76", "v77", "v78", "v79"

DI unsigned cvtpk(float lo, float hi) { f32x2 v = {lo, hi}; bf2 b = __builtin_convertvector(v, bf2); return __builtin_bit_cast(unsigned, b); }
DI float bflo(unsigned u) { return __uint_as_float(u << 16); }
DI float bfhi(unsigned u) { return __uint_as_float(u & 0xffff0000u); }
DI u32x4 pack8(const f32x16& x, int s, float m) {
  u32x4 p = {cvtpk(x[8*s] * m, x[8*s+1] * m), cvtpk(x[8*s+2] * m, x[8*s+3] * m), cvtpk(x[8*s+4] * m, x[8*s+5] * m), cvtpk(x[8*s+6] * m, x[8*s+7] * m)};
  return p;
}
template <int CTRL, int RM> DI float dpp_add(float v) { return v + __int_as_float(__builtin_amdgcn_update_dpp(0, __float_as_int(v), CTRL, RM, 0xf, true)); }
DI float wave_sum(float v) {
  v = dpp_add<0x111, 0xf>(v); v = dpp_add<0x112, 0xf>(v); v = dpp_add<0x114, 0xf>(v); v = dpp_add<0x118, 0xf>(v);
  v = dpp_add<0x142, 0xa>(v); v = dpp_add<0x143, 0xc>(v);
  return __int_as_float(__builtin_amdgcn_readlane(__float_as_int(v), 63));
}
DI void wave_sum2(float& a, float& b) {
  a = dpp_add<0x111, 0xf>(a); b = dpp_add<0x111, 0xf>(b); a = dpp_add<0x112, 0xf>(a); b = dpp_add<0x112, 0xf>(b);
  a = dpp_add<0x114, 0xf>(a); b = dpp_add<0x114, 0xf>(b); a = dpp_add<0x118, 0xf>(a); b = dpp_add<0x118, 0xf>(b);
  a = dpp_add<0x142, 0xa>(a); b = dpp_add<0x142, 0xa>(b); a = dpp_add<0x143, 0xc>(a); b = dpp_add<0x143, 0xc>(b);
  a = __int_as_float(__builtin_amdgcn_readlane(__float_as_int(a), 63)); b = __int_as_float(__builtin_amdgcn_readlane(__float_as_int(b), 63));
}
template <int CTRL, int RM> DI unsigned dpp_maxu(unsigned v) { const unsigned o = __builtin_amdgcn_update_dpp(v, v, CTRL, RM, 0xf, false); return o > v ? o : v; }
DI float wave_max(float x) {
  unsigned v = __float_as_uint(x);
  v = dpp_maxu<0x111, 0xf>(v); v = dpp_maxu<0x112, 0xf>(v); v = dpp_maxu<0x114, 0xf>(v); v = dpp_maxu<0x118, 0xf>(v);
  v = dpp_maxu<0x142, 0xa>(v); v = dpp_maxu<0x143, 0xc>(v);
  return __uint_as_float(__builtin_amdgcn_readlane(v, 63));
}
DI float half_swap_add(float v) { auto r = __builtin_amdgcn_permlane32_swap(__float_as_uint(v), __float_as_uint(v), false, false); return __uint_as_float(r[0]) + __uint_as_float(r[1]); }
DI float half_swap_max(float v) { auto r = __builtin_amdgcn_permlane32_swap(__float_as_uint(v), __float_as_uint(v), false, false); return fmaxf(__uint_as_float(r[0]), __uint_as_float(r[1])); }
DI void glds16(const void* sbase, unsigned voff, unsigned lds) {
  unsigned sv;
  asm volatile("s_mov_b32 %0, m0\n\ts_mov_b32 m0, %3\n\ts_nop 0\n\tglobal_load_lds_dwordx4 %1, %2\n\ts_mov_b32 m0, %0" : "=&s"(sv) : "v"(voff), "s"(sbase), "s"(lds) : "memory");
}
typedef short v4i16_t __attribute__((ext_vector_type(4)));
DI s16x4 trread(const LAS char* p) { return __builtin_bit_cast(s16x4, __builtin_amdgcn_ds_read_tr16_b64_v4i16((LAS v4i16_t*)p)); }
DI bf16x8 cat4(s16x4 a, s16x4 b) { return __builtin_shufflevector(a, b, 0, 1, 2, 3, 4, 5, 6, 7); }
#define RAWBAR() asm volatile("s_waitcnt lgkmcnt(0)\n\ts_barrier" ::: "memory")


typedef __amdgpu_buffer_rsrc_t rsrc_t;
DI void st16(rsrc_t r, unsigned off, u32x4 v) { __builtin_amdgcn_raw_buffer_store_b128(v, r, off, 0, 16); }
DI void st4(rsrc_t r, unsigned off, unsigned v) { __builtin_amdgcn_raw_buffer_store_b32(v, r, off, 0, 16); }
DI u32x4 ld16(rsrc_t r, unsigned off) { return __builtin_amdgcn_raw_buffer_load_b128(r, off, 0, 16); }
DI unsigned ld4(rsrc_t r, unsigned off) { return __builtin_amdgcn_raw_buffer_load_b32(r, off, 0, 16); }
DI void publish(char* ws, unsigned off, u64 tag, int lane) {
  asm volatile("s_waitcnt vmcnt(0)" ::: "memory");
  if (lane == 0) __hip_atomic_store((GAS u64*)(ws + off), tag, __ATOMIC_RELAXED, __HIP_MEMORY_SCOPE_AGENT);
}
template <int SLEEP>
DI void poll(const char* ws, unsigned off, u64 tag) {
#pragma unroll 1
  for (unsigned spins = 0; spins < (1u << 18); ++spins) {
    const u64 v = __hip_atomic_load((const GAS u64*)(ws + off), __ATOMIC_RELAXED, __HIP_MEMORY_SCOPE_AGENT);
    if (__all(v == tag)) break;
    __builtin_amdgcn_s_sleep(SLEEP);
  }
  __builtin_amdgcn_fence(__ATOMIC_ACQUIRE, "wavefront");
  asm volatile("" ::: "memory");
}
DI void poll3(const char* ws, unsigned o0, unsigned o1, unsigned o2, u64 tag) {
#pragma unroll 1
  for (unsigned spins = 0; spins < (1u << 18); ++spins) {
    const u64 a = __hip_atomic_load((const GAS u64*)(ws + o0), __ATOMIC_RELAXED, __HIP_MEMORY_SCOPE_AGENT);
    const u64 b = __hip_atomic_load((const GAS u64*)(ws + o1), __ATOMIC_RELAXED, __HIP_MEMORY_SCOPE_AGENT);
    const u64 c = __hip_atomic_load((const GAS u64*)(ws + o2), __ATOMIC_RELAXED, __HIP_MEMORY_SCOPE_AGENT);
    if (__all(a == tag && b == tag && c == tag)) break;
    __builtin_amdgcn_s_sleep(4);
  }
  __builtin_amdgcn_fence(__ATOMIC_ACQUIRE, "wavefront");
  asm volatile("" ::: "memory");
}
DI void lds_arrive(char* lds, int word, int lane) {
  asm volatile("s_waitcnt lgkmcnt(0)" ::: "memory");
  if (lane == 0) __hip_atomic_fetch_add((LAS unsigned*)((LAS char*)lds + L_MISC) + word, 1u, __ATOMIC_RELAXED, __HIP_MEMORY_SCOPE_WORKGROUP);
  asm volatile("" ::: "memory");
}
template <int SLEEP = 1>
DI void lds_wait(char* lds, int word, unsigned n) {
#pragma unroll 1
  for (unsigned spins = 0; spins < (1u << 22); ++spins) {
    const unsigned v = __hip_atomic_load((LAS unsigned*)((LAS char*)lds + L_MISC) + word, __ATOMIC_RELAXED, __HIP_MEMORY_SCOPE_WORKGROUP);
    if (__builtin_amdgcn_readfirstlane(v) >= n) break;
    __builtin_amdgcn_s_sleep(SLEEP);
  }
  asm volatile("" ::: "memory");
}
DI void lds_init(char* lds, int lane) {
  if (lane < 16) *(LAS unsigned*)((LAS char*)lds + L_MISC + (C_FIRST + lane) * 4) = 0u;
  if (lane < 16) *(LAS unsigned*)((LAS char*)lds + L_CONST + (lane >> 3) * 512 + (lane & 7) * 4) = (lane & 4) ? 0u : 0x3F803F80u;
}
DI void epoch_fetch(const char* ws, char* lds, int lane) {
  if (lane < 2) {
    unsigned sv; const unsigned dst = (unsigned)(size_t)lds + L_MISC + M_TAG * 4, vo = lane * 4; const char* src = ws + WS_E;
    asm volatile("s_mov_b32 %0, m0\n\ts_mov_b32 m0, %3\n\ts_nop 0\n\tglobal_load_lds_dword %1, %2 sc1\n\ts_mov_b32 m0, %0" : "=&s"(sv) : "v"(vo), "s"(src), "s"(dst) : "memory");
  }
}
DI u64 epoch_tag(char* lds) {
  const u32x2 e = *(const LAS u32x2*)((const LAS char*)lds + L_MISC + M_TAG * 4);
  return ((u64)__builtin_amdgcn_readfirstlane(e[0]) | ((u64)__builtin_amdgcn_readfirstlane(e[1]) << 32)) + 1;
}
DI void touch_lines(const char* sbase, unsigned voff, char* lds) {
  unsigned sv; const unsigned dst = (unsigned)(size_t)lds + L_MISC + 512;
  asm volatile("s_mov_b32 %0, m0\n\ts_mov_b32 m0, %3\n\ts_nop 0\n\tglobal_load_lds_dword %1, %2\n\ts_mov_b32 m0, %0" : "=&s"(sv) : "v"(voff), "s"(sbase), "s"(dst) : "memory");
}
DI void lds_sync(char* lds, int word, int lane) { lds_arrive(lds, word, lane); lds_wait(lds, word, 4); }

constexpr float QSCALE = 0.25f * 1.4426950408889634f;

DI void qkv_store(const f32x16& acc, bool isv, int col0, int t32, rsrc_t wr, char* ws, u64 tag, int lane) {
  const int r = lane & 31, hh = lane >> 5, hp = (col0 & 511) >> 5;
  if (isv) {
    const unsigned vb = WS_KV + (unsigned)(2 * hp + (r >> 4)) * KVH + t32 * KVT + 1024 + hh * 256 + (r & 15) * 16;
    st16(wr, vb, pack8(acc, 0, 1.f));
    st16(wr, vb + 512, pack8(acc, 1, 1.f));
  } else {
    const bool isq = col0 < 512;
    const float sc = isq ? QSCALE : 1.f;
#pragma unroll
    for (int e = 0; e < 2; ++e) {
      const unsigned tile = (2 * hp + e) * 64 + t32;
      st16(wr, isq ? WS_Q + (tile * 64 + lane) * 16 : WS_KV + (unsigned)(2 * hp + e) * KVH + t32 * KVT + lane * 16, pack8(acc, e, sc));
      float ss = 0.f;
#pragma unroll
      for (int i = 0; i < 8; ++i) ss = fmaf(acc[8 * e + i], acc[8 * e + i], ss);
      ss = wave_max(half_swap_add(ss)) * (sc * sc);
      if (lane == 0) st4(wr, (isq ? WS_QN : WS_KN) + tile * 4, __float_as_uint(ss));
    }
  }
}

#define P1_PRE(NAME) do { unsigned m0s_, cnt_, dst_ = lds0; \
  asm volatile(NAME##_ASM : "+{s[40:41]}"(xs), "+{s[42:43]}"(wsrc), [dst] "+s"(dst_), [m0s] "=&s"(m0s_), [cnt] "=&s"(cnt_) \
    : [vo0] "v"(vo[0]), [vo1] "v"(vo[1]), [vo2] "v"(vo[2]), [vo3] "v"(vo[3]), [vo4] "v"(vo[4]), [xrel] "s"(xrel), [wrel] "s"(wrel) : "scc", "memory"); } while (0)
#define P1_OPS "+{s[40:41]}"(xs), "+{s[42:43]}"(wsrc), [dst] "+s"(dst_), [xa0] "+v"(xa[0]), [xa1] "+v"(xa[1]), [xa2] "+v"(xa[2]), [xa3] "+v"(xa[3]), [wa] "+v"(wa), \
    [m0s] "=&s"(m0s_), [s] "=&s"(s_), [sl] "=&s"(sl_), [dl] "=&s"(dl_)
#define P1_INS [vo0] "v"(vo[0]), [vo1] "v"(vo[1]), [vo2] "v"(vo[2]), [vo3] "v"(vo[3]), [vo4] "v"(vo[4]), [xrel] "s"(xrel), [wrel] "s"(wrel)
template <int ROLE, bool ISV>
DI void p1_gemm(char* ws, rsrc_t wr, char* lds, int rb, int cb, int w4, int lane, u64 tag, const unsigned (&vo)[5], const char* xs, const char* wsrc, unsigned xrel, unsigned wrel) {
  constexpr int NJ = ROLE ? 1 : 2, J0 = ROLE ? 2 : 0;
  const int r = lane & 31, hh = lane >> 5;
  const unsigned lds0 = (unsigned)(size_t)lds;
  unsigned xa[4];
#pragma unroll
  for (int st = 0; st < 4; ++st) xa[st] = lds0 + (32 * w4 + r) * 128 + (((2 * st + hh) ^ ((r >> 1) & 7)) * 16);
  unsigned wa = lds0 + 16384 + (8 * hh + ((lane & 15) >> 2)) * 192 + (16 * ((lane >> 4) & 1) + 4 * (lane & 3)) * 2 + J0 * 64;
  f32x16 acc[NJ];
  unsigned m0s_, s_, sl_, dl_, dst_ = lds0 + 4 * P1_SLOT;
  if constexpr (ROLE == 0 && !ISV) asm volatile(PAN_ASM : "=&{v[0:15]}"(acc[0]), "=&{v[16:31]}"(acc[NJ - 1]), P1_OPS : P1_INS : PAN_CLOB, "scc", "memory");
  if constexpr (ROLE == 0 && ISV)  asm volatile(PAV_ASM : "=&{v[0:15]}"(acc[0]), "=&{v[16:31]}"(acc[NJ - 1]), P1_OPS : P1_INS : PAV_CLOB, "scc", "memory");
  if constexpr (ROLE == 1 && !ISV) asm volatile(PBN_ASM : "=&{v[0:15]}"(acc[0]), P1_OPS : P1_INS : PBN_CLOB, "scc", "memory");
  if constexpr (ROLE == 1 && ISV)  asm volatile(PBV_ASM : "=&{v[0:15]}"(acc[0]), P1_OPS : P1_INS : PBV_CLOB, "scc", "memory");
  if (ROLE == 1 && lane == 0)
    __hip_atomic_store((GAS u64*)(ws + WS_F2 + (w4 * 256 + blockIdx.x) * 8), tag, __ATOMIC_RELAXED, __HIP_MEMORY_SCOPE_AGENT);
  const int t32 = rb * 4 + w4;
#pragma unroll
  for (int j = 0; j < NJ; ++j) qkv_store(acc[j], ISV, 96 * cb + 32 * (J0 + j), t32, wr, ws, tag, lane);
  asm volatile("s_waitcnt vmcnt(0)" ::: "memory");
  if (lane < NJ) __hip_atomic_store((GAS u64*)(ws + WS_F1 + ((3 * cb + J0 + lane) * 64 + t32) * 8), tag, __ATOMIC_RELAXED, __HIP_MEMORY_SCOPE_AGENT);
}

DI void ln_row(char* lds, int wave, int lane) {
  const u32x4 lr = *(const LAS u32x4*)((const LAS char*)lds + L_LN + wave * 1024 + lane * 16), gw = *(const LAS u32x4*)((const LAS char*)lds + L_LN + 8192 + lane * 16);
  float f[8], d = 0.f;
#pragma unroll
  for (int i = 0; i < 4; ++i) { f[2 * i] = bflo(lr[i]); f[2 * i + 1] = bfhi(lr[i]); }
#pragma unroll
  for (int i = 0; i < 4; ++i) { d = fmaf(f[2 * i], bflo(gw[i]), d); d = fmaf(f[2 * i + 1], bfhi(gw[i]), d); }
  float sum = ((f[0] + f[1]) + (f[2] + f[3])) + ((f[4] + f[5]) + (f[6] + f[7]));
  wave_sum2(sum, d);
  const float mean = sum * (1.f / 512.f);
  float sq = 0.f;
#pragma unroll
  for (int i = 0; i < 8; ++i) { f[i] -= mean; sq = fmaf(f[i], f[i], sq); }
  const float rstd = __builtin_amdgcn_rsqf(wave_sum(sq) * (1.f / 512.f) + 1e-5f);
  u32x4 p;
#pragma unroll
  for (int i = 0; i < 4; ++i) p[i] = cvtpk(f[2 * i] * rstd, f[2 * i + 1] * rstd);
  *(LAS u32x4*)((LAS char*)lds + L_LN + wave * 1024 + lane * 16) = p;
  if (lane == 0) *(LAS float*)((LAS char*)lds + L_MISC + (M_G + wave) * 4) = __builtin_amdgcn_rcpf(1.f + __builtin_amdgcn_exp2f(d * -1.4426950408889634f));
}

DI void phase1(const u16* x, const u16* wqkv, const u16* gatew, char* ws, rsrc_t wr, char* lds, int wave, int lane, u64& tag) {
  const int xcd = blockIdx.x & 7, loc = blockIdx.x >> 3, w4 = wave & 3;
  const int rb = (xcd >> 1) * 4 + (loc & 3), cb = (xcd & 1) * 8 + (loc >> 2);
  const unsigned lds0 = (unsigned)(size_t)lds;
  const char* xs = (const char*)x + (size_t)rb * 128 * 1024; const char* wsrc = (const char*)wqkv + cb * 192;
  const int s0 = blockIdx.x * 8;
  glds16((const char*)x + (size_t)(s0 + wave) * 1024, lane * 16, lds0 + L_LN + wave * 1024);
  glds16(gatew, lane * 16, lds0 + L_LN + 8192);
  unsigned vo[5];
#pragma unroll
  for (int i = 0; i < 2; ++i) { const int row = 16 * wave + 8 * i + (lane >> 3); vo[i] = row * 1024 + (((lane & 7) ^ ((row >> 1) & 7)) * 16) - i * 1024; }
  const unsigned xrel = wave * 2048;
  if (wave < 4) {
    vo[2] = vo[3] = vo[4] = 0;
    const unsigned wrel = 0;
    if (wave == 0) { epoch_fetch(ws, lds, lane); lds_init(lds, lane); }
    P1_PRE(PAP);
    asm volatile("s_waitcnt vmcnt(8)" ::: "memory");
    ln_row(lds, wave, lane);
    RAWBAR();
    tag = epoch_tag(lds);
    if (cb <= 10) p1_gemm<0, false>(ws, wr, lds, rb, cb, w4, lane, tag, vo, xs, wsrc, xrel, wrel);
    else p1_gemm<0, true>(ws, wr, lds, rb, cb, w4, lane, tag, vo, xs, wsrc, xrel, wrel);
  } else {
#pragma unroll
    for (int i = 0; i < 3; ++i) { const int n = 64 * (3 * w4 + i) + lane, row = n / 12, c = n - row * 12; vo[2 + i] = row * 3072 + c * 16 - i * 1024; }
    const unsigned wrel = 16384 + w4 * 3072;
    P1_PRE(PBP);
    asm volatile("s_waitcnt vmcnt(20)" ::: "memory");
    ln_row(lds, wave, lane);
    RAWBAR();
    tag = epoch_tag(lds);
    u32x4 col[2];
#pragma unroll
    for (int e = 0; e < 2; ++e) {
      const LAS u16* p = (const LAS u16*)((const LAS char*)lds + L_LN) + w4 * 128 + e * 64 + lane;
#pragma unroll
      for (int i = 0; i < 4; ++i) { const u16x2 v = {p[(2 * i) * 512], p[(2 * i + 1) * 512]}; col[e][i] = __builtin_bit_cast(unsigned, v); }
    }
    const unsigned gv = *(const LAS unsigned*)((const LAS char*)lds + L_MISC + (M_G + (lane & 7)) * 4);
#pragma unroll
    for (int e = 0; e < 2; ++e) {
      const int d = w4 * 128 + e * 64 + lane;
      st16(wr, WS_H + (((d >> 5) * 128 + (s0 >> 4)) * 64 + ((s0 >> 3) & 1) * 32 + (d & 31)) * 16, col[e]);
    }
    if (lane < 8) st4(wr, WS_G + (w4 * 2048 + s0 + lane) * 4, gv);
    if (cb <= 9) p1_gemm<1, false>(ws, wr, lds, rb, cb, w4, lane, tag, vo, xs, wsrc, xrel, wrel);
    else p1_gemm<1, true>(ws, wr, lds, rb, cb, w4, lane, tag, vo, xs, wsrc, xrel, wrel);
  }
}

DI void attn_slow(char* ws, rsrc_t wr, char* lds, int head, int qb, int w4, int lane, u64 tag) {
  const int r = lane & 31, hh = lane >> 5;
  const unsigned kvb = WS_KV + (unsigned)head * KVH + w4 * 16 * KVT, vo = 1024 + hh * 256 + (r & 15) * 16;
  const unsigned cv = (r == 16 || r == 20) ? 0x3F803F80u : 0u; const u32x4 cc = {cv, cv, cv, cv};
  for (int half = 0; half < 2; ++half) {
    for (int i = 0; i < 4; ++i) {
      const int n = 2 * i + half;
      const bf16x8 q = __builtin_bit_cast(bf16x8, ld16(wr, WS_Q + ((head * 64 + qb * 8 + n) * 64 + lane) * 16));
      float m = -INFINITY; f32x16 acc, z;
#pragma unroll
      for (int e = 0; e < 16; ++e) { acc[e] = 0.f; z[e] = 0.f; }
      for (int t = 0; t < 16; ++t) {
        const bf16x8 kf = __builtin_bit_cast(bf16x8, ld16(wr, kvb + t * KVT + lane * 16));
        const u32x4 l0 = ld16(wr, kvb + t * KVT + vo), l1 = ld16(wr, kvb + t * KVT + vo + 512);
        const bf16x8 v0 = __builtin_bit_cast(bf16x8, r < 16 ? l0 : cc), v1 = __builtin_bit_cast(bf16x8, r < 16 ? l1 : cc);
        f32x16 s = MFMA(kf, q, z);
        float tm = s[0];
#pragma unroll
        for (int e = 1; e < 16; ++e) tm = fmaxf(tm, s[e]);
        tm = half_swap_max(tm);
        const float mn = fmaxf(m, tm), al = __builtin_amdgcn_exp2f(m - mn);
        m = mn;
#pragma unroll
        for (int e = 0; e < 16; ++e) { s[e] = __builtin_amdgcn_exp2f(s[e] - mn); acc[e] *= al; }
        acc = MFMA(v0, __builtin_bit_cast(bf16x8, pack8(s, 0, 1.f)), acc);
        acc = MFMA(v1, __builtin_bit_cast(bf16x8, pack8(s, 1, 1.f)), acc);
      }
      LAS char* dst = (LAS char*)lds + L_X + (i * 4 + w4) * 2560;
      const f32x4 a = {acc[0], acc[1], acc[2], acc[3]}, b = {acc[4], acc[5], acc[6], acc[7]};
      *(LAS f32x4*)(dst + lane * 16) = a; *(LAS f32x4*)(dst + 1024 + lane * 16) = b;
      *(LAS float*)(dst + 2048 + lane * 4) = acc[8]; *(LAS float*)(dst + 2304 + lane * 4) = m;
    }
    lds_sync(lds, half ? C_S2 : C_S0, lane);
    {
      float ms[4], mx = -INFINITY;
#pragma unroll
      for (int k = 0; k < 4; ++k) { ms[k] = *(const LAS float*)((const LAS char*)lds + L_X + (w4 * 4 + k) * 2560 + 2304 + lane * 4); mx = fmaxf(mx, ms[k]); }
      f32x16 o;
#pragma unroll
      for (int e = 0; e < 16; ++e) o[e] = 0.f;
#pragma unroll
      for (int k = 0; k < 4; ++k) {
        const LAS char* src = (const LAS char*)lds + L_X + (w4 * 4 + k) * 2560; const float f = __builtin_amdgcn_exp2f(ms[k] - mx);
        const f32x4 a = *(const LAS f32x4*)(src + lane * 16), b = *(const LAS f32x4*)(src + 1024 + lane * 16);
#pragma unroll
        for (int e = 0; e < 4; ++e) { o[e] = fmaf(a[e], f, o[e]); o[4 + e] = fmaf(b[e], f, o[4 + e]); }
        o[8] = fmaf(*(const LAS float*)(src + 2048 + lane * 4), f, o[8]);
      }
      st16(wr, WS_O + (((qb * 8 + 2 * w4 + half) * 32 + head) * 64 + lane) * 16, pack8(o, 0, __builtin_amdgcn_rcpf(o[8])));
    }
    lds_sync(lds, half ? C_S3 : C_S1, lane);
  }
}

DI void attention(char* ws, rsrc_t wr, char* lds, int w4, int lane, u64 tag) {
  const int xcd = blockIdx.x & 7, loc = blockIdx.x >> 3, head = xcd * 4 + (loc >> 3), qb = loc & 7, hp = head >> 1;
  const int r = lane & 31, hh = lane >> 5;
  if (w4 == 0) {
    poll3(ws, WS_F1 + (hp * 64 + qb * 8 + (lane & 7)) * 8, WS_F1 + ((16 + hp) * 64 + lane) * 8, WS_F1 + ((32 + hp) * 64 + lane) * 8, tag);
    lds_arrive(lds, C_H1, lane);
  } else lds_wait(lds, C_H1, 1);
  const unsigned kn = ld4(wr, WS_KN + (head * 64 + lane) * 4), qn = ld4(wr, WS_QN + (head * 64 + qb * 8 + (lane & 7)) * 4);
  u32x4 qv[8];
#pragma unroll
  for (int n = 0; n < 8; ++n) qv[n] = ld16(wr, WS_Q + ((head * 64 + qb * 8 + n) * 64 + lane) * 16);
  const char* kvp = ws + WS_KV + (size_t)head * KVH + w4 * 16 * KVT;
  unsigned slot = (unsigned)(size_t)lds + L_AR + w4 * 8192;
  const unsigned dvo = lane * 16;
  f32x16 o[8];
  { unsigned m0s; asm volatile(ATTP_ASM : ATTP_OUTS : ATTP_INS : ATTP_CLOB); }
  lds_arrive(lds, C_AP, lane);
  bf16x8 q[8];
#pragma unroll
  for (int n = 0; n < 8; ++n) q[n] = __builtin_bit_cast(bf16x8, qv[n]);
  const float M = __builtin_sqrtf(wave_max(__uint_as_float(kn)) * wave_max(__uint_as_float(qn))) * 1.02f;
  if (__builtin_expect(M <= 48.f, 1)) {
    f32x16 negm;
#pragma unroll
    for (int i = 0; i < 16; ++i) negm[i] = -M;
    unsigned ka = slot + lane * 16, cnt, inc, rem, m0s;
    unsigned va = r < 16 ? slot + 1024 + hh * 256 + r * 16 : (unsigned)(size_t)lds + L_CONST + ((r == 16 || r == 20) ? 0 : 16);
    const unsigned xm1 = r < 16 ? 2048 : 0, xm2 = r < 16 ? 4096 : 0;
    __builtin_amdgcn_s_setprio(1);
    asm volatile(ATT_ASM : ATT_OUTS : ATT_INS : ATT_CLOB);
    const int ln = __builtin_amdgcn_mbcnt_hi(~0u, __builtin_amdgcn_mbcnt_lo(~0u, 0u));
    lds_arrive(lds, C_OD, ln);
    float m[2][9];
#pragma unroll
    for (int p = 0; p < 4; ++p) {
      if (p != w4) {
        LAS char* dst = (LAS char*)lds + L_X + (p * 6 + ((w4 - p - 1) & 3)) * 2304;
#pragma unroll
        for (int e = 0; e < 2; ++e) {
          const f32x16& v = o[2 * p + e];
          const f32x4 a = {v[0], v[1], v[2], v[3]}, b = {v[4], v[5], v[6], v[7]};
          *(LAS f32x4*)(dst + e * 6912 + ln * 16) = a; *(LAS f32x4*)(dst + e * 6912 + 1024 + ln * 16) = b; *(LAS float*)(dst + e * 6912 + 2048 + ln * 4) = v[8];
        }
      } else {
#pragma unroll
        for (int e = 0; e < 2; ++e)
#pragma unroll
          for (int i = 0; i < 9; ++i) m[e][i] = o[2 * p + e][i];
      }
    }
    lds_sync(lds, C_OA, ln);
#pragma unroll
    for (int e = 0; e < 2; ++e) {
#pragma unroll
      for (int k = 0; k < 3; ++k) {
        const LAS char* src = (const LAS char*)lds + L_X + (w4 * 6 + e * 3 + k) * 2304;
        const f32x4 a = *(const LAS f32x4*)(src + ln * 16), b = *(const LAS f32x4*)(src + 1024 + ln * 16);
#pragma unroll
        for (int i = 0; i < 4; ++i) { m[e][i] += a[i]; m[e][4 + i] += b[i]; }
        m[e][8] += *(const LAS float*)(src + 2048 + ln * 4);
      }
      const float rl = __builtin_amdgcn_rcpf(m[e][8]);
      const u32x4 pk = {cvtpk(m[e][0] * rl, m[e][1] * rl), cvtpk(m[e][2] * rl, m[e][3] * rl), cvtpk(m[e][4] * rl, m[e][5] * rl), cvtpk(m[e][6] * rl, m[e][7] * rl)};
      st16(wr, WS_O + (((qb * 8 + 2 * w4 + e) * 32 + head) * 64 + ln) * 16, pk);
    }
    publish(ws, WS_F4 + ((qb * 4 + w4) * 32 + head) * 8, tag, ln);
  } else {
    asm volatile("s_waitcnt vmcnt(0)" ::: "memory");
    attn_slow(ws, wr, lds, head, qb, w4, lane, tag);
    lds_arrive(lds, C_OD, lane);
    publish(ws, WS_F4 + ((qb * 4 + w4) * 32 + head) * 8, tag, lane);
  }
}

DI void core_lane_w(unsigned (&wo)[4], int lane, unsigned stride) {
#pragma unroll
  for (int i = 0; i < 4; ++i) wo[i] = (8 * i + (lane >> 3)) * stride + (((lane & 7) ^ (((lane >> 4) & 1) << 2)) * 16) - i * 1024;
}
DI void core_lane_lr(unsigned (&lr)[2], int lane, unsigned ring, bool permuted) {
  const int hh = lane >> 5, blk = (lane >> 4) & 1, q = (lane & 15) >> 2, p = lane & 3, sw = (q >> 1) & 1;
  const int rowb = (permuted ? 4 * hh : 8 * hh) + q;
#pragma unroll
  for (int i = 0; i < 2; ++i) lr[i] = ring + rowb * 128 + ((i ^ sw) * 64) + 32 * blk + 8 * p;
}
#define CORE_PRE(NAME, wptr, wo, ring) do { unsigned m0s_; \
  asm volatile(NAME##_ASM : "+{s[40:41]}"(wptr), [m0s] "=&s"(m0s_) \
    : [wo0] "v"(wo[0]), [wo1] "v"(wo[1]), [wo2] "v"(wo[2]), [wo3] "v"(wo[3]), [ldsb] "s"(ring) : "scc", "memory"); } while (0)
#define CORE_CALL(NAME, acc, wptr, xptr, wo, xo, lr, ring, trips) do { unsigned m0s_, cnt_ = (trips); \
  asm volatile(NAME##_ASM : "=&{v[0:15]}"(acc[0]), "=&{v[16:31]}"(acc[1]), "=&{v[32:47]}"(acc[2]), "=&{v[48:63]}"(acc[3]), "+{s[40:41]}"(wptr), "+{s[42:43]}"(xptr), [m0s] "=&s"(m0s_), [cnt] "+s"(cnt_) \
    : [wo0] "v"(wo[0]), [wo1] "v"(wo[1]), [wo2] "v"(wo[2]), [wo3] "v"(wo[3]), [xo0] "v"(xo[0]), [xo1] "v"(xo[1]), [lr0] "v"(lr[0]), [lr1] "v"(lr[1]), [ldsb] "s"(ring) \
    : NAME##_CLOB, "scc", "memory"); } while (0)

DI void reduce4(f32x16 (&acc)[4], f32x16& mine, char* lds, int w4, int lane, int cb) {
#pragma unroll
  for (int t = 0; t < 4; ++t) {
    if (w4 != t) {
      LAS char* dst = (LAS char*)lds + L_RING + w4 * G_RING + ((t - w4 - 1) & 3) * 4096 + lane * 16;
#pragma unroll
      for (int g = 0; g < 4; ++g) { const f32x4 v = {acc[t][4 * g], acc[t][4 * g + 1], acc[t][4 * g + 2], acc[t][4 * g + 3]}; *(LAS f32x4*)(dst + g * 1024) = v; }
    } else mine = acc[t];
  }
  lds_sync(lds, cb, lane);
#pragma unroll
  for (int k = 1; k < 4; ++k) {
    const LAS char* src = (const LAS char*)lds + L_RING + ((w4 + k) & 3) * G_RING + (3 - k) * 4096 + lane * 16;
#pragma unroll
    for (int g = 0; g < 4; ++g) { const f32x4 v = *(const LAS f32x4*)(src + g * 1024);
#pragma unroll
      for (int i = 0; i < 4; ++i) mine[4 * g + i] += v[i]; }
  }
}
DI float gelu_tanh(float v) {
  constexpr float c1 = 2.f * 1.4426950408889634f * 0.7978845608028654f, c3 = c1 * 0.044715f;
  const float e = __builtin_amdgcn_exp2f(v * fmaf(v * v, c3, c1));
  return fmaf(-v, __builtin_amdgcn_rcpf(e + 1.f), v);
}

DI void mlp_chain(const u16* w1, const u16* b1, const u16* w2, const u16* b2, const u16* wo_w, u16* out, char* ws, rsrc_t wr, char* lds, int w4, int lane, u64 tag) {
  const int xcd = blockIdx.x & 7, loc = blockIdx.x >> 3, r = lane & 31, hh = lane >> 5;
  __builtin_amdgcn_s_setprio(2);
  const unsigned ring = (unsigned)(size_t)lds + L_RING + w4 * G_RING;
  unsigned wo[4], lr[2], xo[2];
  f32x16 acc[4];
  const int sb = xcd * 4 + (loc >> 3), db = loc & 7, srow = sb * 64 + (w4 & 1) * 32 + r;
  if (loc < 28) {
    const int idx = xcd * 28 + loc, fb = idx >> 3, db = idx & 7;
    core_lane_w(wo, lane, 3584); core_lane_lr(lr, lane, ring, false);
    xo[0] = lane * 16; xo[1] = lane * 16 + 128 * 1024;
    const char* wptr = (const char*)w1 + (size_t)(512 * w4) * 3584 + fb * 128;
    const char* xptr = ws + WS_H + (size_t)(2 * db * 128 + 32 * w4) * 1024;
    const int f0 = fb * 64 + (w4 >> 1) * 32, dt = 2 * db + (w4 & 1);
    u32x2 bb[4];
#pragma unroll
    for (int g = 0; g < 4; ++g) bb[g] = *(const u32x2*)(b1 + f0 + 8 * g + 4 * hh);
    poll<8>(ws, WS_F2 + ((db >> 1) * 256 + 64 * w4 + lane) * 8, tag);
    lds_wait(lds, C_AP, 4);
    CORE_PRE(G1P, wptr, wo, ring);
    CORE_CALL(G1, acc, wptr, xptr, wo, xo, lr, ring, 3);
    f32x16 mine;
    reduce4(acc, mine, lds, w4, lane, C_R1B);
    lds_arrive(lds, C_R1C, lane);
#pragma unroll
    for (int g = 0; g < 4; ++g) {
      mine[4 * g] = gelu_tanh(mine[4 * g] + bflo(bb[g][0])); mine[4 * g + 1] = gelu_tanh(mine[4 * g + 1] + bfhi(bb[g][0]));
      mine[4 * g + 2] = gelu_tanh(mine[4 * g + 2] + bflo(bb[g][1])); mine[4 * g + 3] = gelu_tanh(mine[4 * g + 3] + bfhi(bb[g][1]));
    }
#pragma unroll
    for (int e = 0; e < 2; ++e) st16(wr, WS_U + ((dt * 112 + (f0 >> 4) + e) * 64 + lane) * 16, pack8(mine, e, 1.f));
    publish(ws, WS_F3 + ((db * 28 + fb) * 4 + w4) * 8, tag, lane);
    lds_wait(lds, C_R1C, 4);
  }
  else {
    lds_wait(lds, C_AP, 4);
  }
  core_lane_lr(lr, lane, ring, true);
  f32x16 keep;
  float gate, bias;
  {
    core_lane_w(wo, lane, 4096);
    xo[0] = lane * 16; xo[1] = lane * 16 + 112 * 1024;
    const int st0 = 28 * w4;
    const char* wptr = (const char*)w2 + (size_t)(16 * st0) * 4096 + sb * 128;
    const char* xptr = ws + WS_U + (size_t)(2 * db * 112 + st0) * 1024;
    poll<8>(ws, WS_F2 + ((db >> 1) * 256 + sb * 8 + (lane & 7)) * 8, tag);
    gate = __uint_as_float(ld4(wr, WS_G + ((db >> 1) * 2048 + srow) * 4));
    bias = __uint_as_float((unsigned)b2[srow] << 16);
    CORE_PRE(G2P, wptr, wo, ring);
    touch_lines((const char*)w2 + sb * 128, (224 * db + 56 * w4 + (lane < 56 ? lane : 55)) * 4096, lds);
    poll<8>(ws, WS_F3 + ((db * 28 + 7 * w4) * 4 + (lane < 28 ? lane : 0)) * 8, tag);
    CORE_CALL(G2, acc, wptr, xptr, wo, xo, lr, ring, 2);
    reduce4(acc, keep, lds, w4, lane, C_R2B);
    lds_arrive(lds, C_R2D, lane);
    const float og = 1.f - gate;
#pragma unroll
    for (int i = 0; i < 16; ++i) keep[i] = og * (keep[i] + bias);
    lds_wait(lds, C_R2D, 4);
  }
  {
    core_lane_w(wo, lane, 1024);
    xo[0] = lane * 16; xo[1] = lane * 16 + 32 * 1024;
    const char* wptr = (const char*)wo_w + (size_t)(128 * w4) * 1024 + db * 128;
    const char* xptr = ws + WS_O + (size_t)(2 * sb * 32 + 8 * w4) * 1024;
    CORE_PRE(G3P, wptr, wo, ring);
    lds_wait<8>(lds, C_OD, 4);
    poll<2>(ws, WS_F4 + (sb * 32 + 8 * w4 + (lane & 7)) * 8, tag);
    CORE_CALL(G3, acc, wptr, xptr, wo, xo, lr, ring, 0);
    f32x16 mine;
    reduce4(acc, mine, lds, w4, lane, C_R3B);
    const int d0 = db * 64 + (w4 >> 1) * 32;
    unsigned p[8];
#pragma unroll
    for (int i = 0; i < 8; ++i) p[i] = cvtpk(fmaf(gate, mine[2 * i], keep[2 * i]), fmaf(gate, mine[2 * i + 1], keep[2 * i + 1]));
#pragma unroll
    for (int k = 0; k < 2; ++k) {
      auto a = __builtin_amdgcn_permlane32_swap(p[4 * k], p[4 * k + 2], false, false);
      auto b = __builtin_amdgcn_permlane32_swap(p[4 * k + 1], p[4 * k + 3], false, false);
      const u32x4 v = {a[0], b[0], a[1], b[1]};
      *(u32x4*)(out + (size_t)srow * 512 + d0 + 16 * k + 8 * hh) = v;
    }
  }
}

__global__ __launch_bounds__(512, 2) void fused(const u16* __restrict__ x, const u16* __restrict__ wqkv, const u16* __restrict__ wo, const u16* __restrict__ w1, const u16* __restrict__ b1,
                                                const u16* __restrict__ w2, const u16* __restrict__ b2, const u16* __restrict__ gatew, char* __restrict__ ws, u16* __restrict__ out) {
  extern __shared__ __attribute__((aligned(16))) char lds[];
  const int lane = threadIdx.x & 63, wave = __builtin_amdgcn_readfirstlane(threadIdx.x >> 6);
  const rsrc_t wr = __builtin_amdgcn_make_buffer_rsrc(ws, 0, WS_END, 0x00020000);
  if (wave == 7 && (blockIdx.x >> 3) * 64 < CODE_LINES) {
    const char* pc; asm volatile("s_getpc_b64 %0" : "=s"(pc));
    const int line = (blockIdx.x >> 3) * 64 + lane;
    touch_lines(pc, (line < CODE_LINES ? line : CODE_LINES - 1) * 128, lds);
  }
  u64 tag;
  phase1(x, wqkv, gatew, ws, wr, lds, wave, lane, tag);
  if (wave < 4) attention(ws, wr, lds, wave, lane, tag);
  else {
    mlp_chain(w1, b1, w2, b2, wo, out, ws, wr, lds, wave - 4, lane, tag);
    if (blockIdx.x == 0 && threadIdx.x == 256) __hip_atomic_store((GAS u64*)(ws + WS_E), tag, __ATOMIC_RELAXED, __HIP_MEMORY_SCOPE_AGENT);
  }
}

extern "C" void kernel_launch(void* const* d_in, const int* in_sizes, int n_in, void* d_out, int out_size, void* d_ws, size_t ws_size, hipStream_t stream) {
  const u16* x = (const u16*)d_in[0]; const u16* wqkv = (const u16*)d_in[1]; const u16* wo = (const u16*)d_in[2];
  const u16* w1 = (const u16*)d_in[4]; const u16* b1 = (const u16*)d_in[5];
  const u16* w2 = (const u16*)d_in[6]; const u16* b2 = (const u16*)d_in[7]; const u16* gw = (const u16*)d_in[8];
  char* ws = (char*)d_ws;
  (void)hipFuncSetAttribute((const void*)fused, hipFuncAttributeMaxDynamicSharedMemorySize, LDS_BYTES);
  fused<<<256, 512, LDS_BYTES, stream>>>(x, wqkv, wo, w1, b1, w2, b2, gw, ws, (u16*)d_out);
}
```
